```python
import torch

def _softmax(x, axis=-1):
    return torch.softmax(x, dim=axis)
def _gelu(x):
    return torch.nn.functional.gelu(x, approximate="tanh")
def _silu(x):
    return torch.nn.functional.silu(x)
def _sigmoid(x):
    return torch.sigmoid(x)
def _layernorm(x, axis=-1, eps=1e-5):
    m = x.mean(dim=axis, keepdim=True)
    v = x.var(dim=axis, keepdim=True, unbiased=False)
    return (x - m) / torch.sqrt(v + eps)
def _scatter_add(tgt, idx, src, axis=0):
    return tgt.clone().index_add_(axis, idx, src)
def _segment_sum(src, seg, n):
    out = torch.zeros((n,) + src.shape[1:], dtype=src.dtype)
    return out.index_add_(0, seg, src)
def _pad(x, pw):
    flat = tuple(v for pair in reversed(pw) for v in pair)
    return torch.nn.functional.pad(x, flat)

kernel_name = 'standalone__gqa_block_cached__sampling'

def setup_inputs():

    _w = (lambda a: (lambda t: t.to(torch.bfloat16) if t.is_floating_point() else t)(torch.as_tensor(a)))
    B, Sq, D = 8, 4, 512
    Hq, Hkv, Sk, Dh = 32, 8, 2048, 128
    assert Hq % Hkv == 0, (Hq, Hkv)
    return {
        "x": _w((lambda *s: torch.randn(*s, dtype=torch.bfloat16) * 0.8)(B, Sq, D)),
        "k_cache": _w((lambda *s: torch.randn(*s, dtype=torch.bfloat16) * 0.8)(B, Hkv, Sk, Dh)),
        "v_cache": _w((lambda *s: torch.randn(*s, dtype=torch.bfloat16) * 0.05)(B, Hkv, Sk, Dh)),
        "wq": _w((lambda *s: torch.randn(*s, dtype=torch.bfloat16) * 0.8)(D, Hq * Dh) * (D ** -0.5)),
        "wo": _w((lambda *s: torch.randn(*s, dtype=torch.bfloat16) * 0.05)(Hq * Dh, D) * ((Hq * Dh) ** -0.5)),
    }

def reference(x, k_cache, v_cache, wq, wo):
    B, Sq, D = x.shape
    Hq, Dh = 32, 128
    Hkv = k_cache.shape[1]
    rep = Hq // Hkv
    q = (lambda x, *d: x.permute(*d))((x @ wq).reshape(B, Sq, Hq, Dh), 0, 2, 1, 3)
    k = (lambda x, n, axis: torch.repeat_interleave(x, n, dim=axis))(k_cache, rep, axis=1)
    v = (lambda x, n, axis: torch.repeat_interleave(x, n, dim=axis))(v_cache, rep, axis=1)
    scores = torch.einsum("bhqd,bhkd->bhqk", q, k) * (Dh ** -0.5)
    p = _softmax(scores, axis=-1)
    o = torch.einsum("bhqk,bhkd->bhqd", p, v)
    return (lambda x, *d: x.permute(*d))(o, 0, 2, 1, 3).reshape(B, Sq, Hq * Dh) @ wo
```

```cpp
#include <hip/hip_runtime.h>
#include <hip/hip_bf16.h>
#include <stdint.h>

typedef unsigned short u16;
__device__ __forceinline__ float bf2f(u16 v) { return __uint_as_float(((unsigned)v) << 16); }
__device__ __forceinline__ u16 f2bf(float f) { __hip_bfloat16 h = __float2bfloat16(f); return *reinterpret_cast<u16*>(&h); }

__global__ void k_q(const u16* x, const u16* wq, u16* q) {
  int n = blockIdx.x * 256 + threadIdx.x;
  int r = blockIdx.y;
  float acc = 0.f;
  for (int k = 0; k < 512; ++k) acc += bf2f(x[r * 512 + k]) * bf2f(wq[(size_t)k * 4096 + n]);
  q[r * 4096 + n] = f2bf(acc);
}
__global__ void k_attn(const u16* q, const u16* kc, const u16* vc, u16* o) {
  int s = blockIdx.x & 3, h = (blockIdx.x >> 2) & 31, b = blockIdx.x >> 7;
  int kvh = h >> 2;
  __shared__ float sq[128];
  __shared__ float sc[2048];
  __shared__ float red[256];
  int t = threadIdx.x;
  if (t < 128) sq[t] = bf2f(q[(b * 4 + s) * 4096 + h * 128 + t]);
  __syncthreads();
  const u16* K = kc + (size_t)(b * 8 + kvh) * 2048 * 128;
  const u16* V = vc + (size_t)(b * 8 + kvh) * 2048 * 128;
  float mx = -INFINITY;
  for (int j = t; j < 2048; j += 256) {
    float a = 0.f;
    for (int d = 0; d < 128; ++d) a += sq[d] * bf2f(K[j * 128 + d]);
    a *= 0.08838834764831845f;
    sc[j] = a; mx = fmaxf(mx, a);
  }
  red[t] = mx; __syncthreads();
  for (int o2 = 128; o2 > 0; o2 >>= 1) { if (t < o2) red[t] = fmaxf(red[t], red[t + o2]); __syncthreads(); }
  mx = red[0]; __syncthreads();
  float sum = 0.f;
  for (int j = t; j < 2048; j += 256) { float e = __expf(sc[j] - mx); sc[j] = e; sum += e; }
  red[t] = sum; __syncthreads();
  for (int o2 = 128; o2 > 0; o2 >>= 1) { if (t < o2) red[t] += red[t + o2]; __syncthreads(); }
  sum = red[0]; __syncthreads();
  int d = t & 127, half = t >> 7;
  float acc = 0.f;
  for (int j = half * 1024; j < half * 1024 + 1024; ++j) acc += sc[j] * bf2f(V[j * 128 + d]);
  red[t] = acc; __syncthreads();
  if (t < 128) o[(b * 4 + s) * 4096 + h * 128 + t] = f2bf((red[t] + red[t + 128]) / sum);
}
__global__ void k_o(const u16* o, const u16* wo, u16* out) {
  int n = blockIdx.x * 64 + (threadIdx.x & 63);
  int r = blockIdx.y;
  int kq = threadIdx.x >> 6;
  __shared__ float red[256];
  float acc = 0.f;
  for (int k = kq * 1024; k < kq * 1024 + 1024; ++k) acc += bf2f(o[r * 4096 + k]) * bf2f(wo[(size_t)k * 512 + n]);
  red[threadIdx.x] = acc; __syncthreads();
  if (threadIdx.x < 64) out[r * 512 + n] = f2bf(red[threadIdx.x] + red[threadIdx.x + 64] + red[threadIdx.x + 128] + red[threadIdx.x + 192]);
}

extern "C" void kernel_launch(void* const* d_in, const int* in_sizes, int n_in,
                              void* d_out, int out_size, void* d_ws, size_t ws_size,
                              hipStream_t stream) {
  const u16* x = (const u16*)d_in[0];
  const u16* kc = (const u16*)d_in[1];
  const u16* vc = (const u16*)d_in[2];
  const u16* wq = (const u16*)d_in[3];
  const u16* wo = (const u16*)d_in[4];
  u16* q = (u16*)d_ws;
  u16* o = q + 32 * 4096;
  k_q<<<dim3(16, 32), 256, 0, stream>>>(x, wq, q);
  k_attn<<<1024, 256, 0, stream>>>(q, kc, vc, o);
  k_o<<<dim3(8, 32), 256, 0, stream>>>(o, wo, (u16*)d_out);
}
```

```cpp
#include <hip/hip_runtime.h>
#include <stdint.h>

typedef unsigned u32;
typedef unsigned long long u64;
typedef unsigned char u8;
typedef u32 v4u __attribute__((ext_vector_type(4)));
typedef u32 v2u __attribute__((ext_vector_type(2)));
typedef float f4 __attribute__((ext_vector_type(4)));
typedef float f2 __attribute__((ext_vector_type(2)));
typedef short s8v __attribute__((ext_vector_type(8)));
typedef __bf16 bf2v __attribute__((ext_vector_type(2)));
#define GAS __attribute__((address_space(1)))
#define LAS __attribute__((address_space(3)))

#define THR 8
#define W_AUX 2
#define STAMPS 0

constexpr u32 WS_EPOCH = 0;
constexpr u32 WS_QBUF = 4096;
constexpr u32 WS_BOX = WS_QBUF + 64 * 16384;
constexpr u32 BOX = 12544;
constexpr u32 WS_END = WS_BOX + 256 * BOX;
constexpr u32 WS_STAMP = 8u << 20;
constexpr u32 L_K = 0;
constexpr u32 L_P = 131072;
constexpr u32 L_QP = L_P, L_Q = L_P + 8192;
constexpr u32 L_M = L_P + 16384, L_L = L_M + 256, L_F = L_L + 256;
constexpr u32 LDS_BYTES = L_F + 256;
enum { F_QP = 0, F_Q, F_K, F_M, F_L, F_P, F_O, F_SENT };

constexpr u32 SENT = 0xFFFFFFFFu;
constexpr int SPIN_G = 1 << 17;
constexpr int SPIN_L = 1 << 22;
constexpr float SCL = 0.08838834764831845f * 1.4426950408889634f;

__device__ __forceinline__ f4 mfma(v4u a, v4u b, f4 c) {
  return __builtin_amdgcn_mfma_f32_16x16x32_bf16(__builtin_bit_cast(s8v, a), __builtin_bit_cast(s8v, b), c, 0, 0, 0);
}
__device__ __forceinline__ u32 pk(float lo, float hi) {
  f2 v = {lo, hi};
  return __builtin_bit_cast(u32, __builtin_convertvector(v, bf2v));
}
__device__ __forceinline__ u32 plo(u32 a, u32 b) { return __builtin_amdgcn_perm(b, a, 0x05040100u); }
__device__ __forceinline__ u32 phi(u32 a, u32 b) { return __builtin_amdgcn_perm(b, a, 0x07060302u); }
__device__ __forceinline__ u32 san(u32 x) { return min(x, 0xFFFFFFFEu); }
__device__ __forceinline__ float fb(u32 x) { return __builtin_bit_cast(float, x); }
__device__ __forceinline__ u32 bf(float x) { return __builtin_bit_cast(u32, x); }
__device__ __forceinline__ u32 umax4(v4u v) { return max(max(v.x, v.y), max(v.z, v.w)); }
__device__ __forceinline__ float xmax(float x) {
  auto s = __builtin_amdgcn_permlane16_swap(bf(x), bf(x), false, false);
  x = fmaxf(fb(s[0]), fb(s[1]));
  auto t = __builtin_amdgcn_permlane32_swap(bf(x), bf(x), false, false);
  return fmaxf(fb(t[0]), fb(t[1]));
}
__device__ __forceinline__ float xsum(float x) {
  auto s = __builtin_amdgcn_permlane16_swap(bf(x), bf(x), false, false);
  x = fb(s[0]) + fb(s[1]);
  auto t = __builtin_amdgcn_permlane32_swap(bf(x), bf(x), false, false);
  return fb(t[0]) + fb(t[1]);
}
__device__ __forceinline__ void ld4_sc1(v4u (&r)[4], const u8* base, u32 voff) {
  asm volatile("global_load_dwordx4 %0, %4, %5 sc1\n\tglobal_load_dwordx4 %1, %4, %5 offset:1024 sc1\n\t"
               "global_load_dwordx4 %2, %4, %5 offset:2048 sc1\n\tglobal_load_dwordx4 %3, %4, %5 offset:3072 sc1\n\ts_waitcnt vmcnt(0)"
               : "=&v"(r[0]), "=&v"(r[1]), "=&v"(r[2]), "=&v"(r[3]) : "v"(voff), "s"(base) : "memory");
}
__device__ __forceinline__ void ld8_sc1(v4u (&r)[8], const u8* base, u32 voff, u32 voff2) {
  asm volatile("global_load_dwordx4 %0, %8, %10 sc1\n\tglobal_load_dwordx4 %1, %8, %10 offset:1024 sc1\n\t"
               "global_load_dwordx4 %2, %8, %10 offset:2048 sc1\n\tglobal_load_dwordx4 %3, %8, %10 offset:3072 sc1\n\t"
               "global_load_dwordx4 %4, %9, %10 sc1\n\tglobal_load_dwordx4 %5, %9, %10 offset:1024 sc1\n\t"
               "global_load_dwordx4 %6, %9, %10 offset:2048 sc1\n\tglobal_load_dwordx4 %7, %9, %10 offset:3072 sc1\n\ts_waitcnt vmcnt(0)"
               : "=&v"(r[0]), "=&v"(r[1]), "=&v"(r[2]), "=&v"(r[3]), "=&v"(r[4]), "=&v"(r[5]), "=&v"(r[6]), "=&v"(r[7])
               : "v"(voff), "v"(voff2), "s"(base) : "memory");
}
__device__ __forceinline__ void ld2_sc1(v4u& a, v4u& b, const u8* base, u32 voff) {
  asm volatile("global_load_dwordx4 %0, %2, %3 sc1\n\tglobal_load_dwordx4 %1, %2, %3 offset:16 sc1\n\ts_waitcnt vmcnt(0)"
               : "=&v"(a), "=&v"(b) : "v"(voff), "s"(base) : "memory");
}
__device__ __forceinline__ void ld1_sc1(v2u& a, const u8* base, u32 voff) {
  asm volatile("global_load_dwordx2 %0, %1, %2 sc1\n\ts_waitcnt vmcnt(0)" : "=&v"(a) : "v"(voff), "s"(base) : "memory");
}
#define CBAR() asm volatile("" ::: "memory")
#define SBAR() __builtin_amdgcn_sched_barrier(0)
template <int N> __device__ __forceinline__ void waitvm() { asm volatile("s_waitcnt vmcnt(%0)" ::"i"(N) : "memory"); }

__device__ __forceinline__ void flag_set(LAS u8* L, int f, int w, int lane) {
  asm volatile("s_waitcnt lgkmcnt(0)" ::: "memory");
  if (lane == 0) *(volatile LAS u32*)(L + L_F + f * 16 + w * 4) = 1u;
  CBAR();
}
__device__ __forceinline__ void flag_wait4(LAS u8* L, int f) {
#pragma nounroll
  for (int i = 0; i < SPIN_L; ++i) {
    v4u v = *(volatile LAS v4u*)(L + L_F + f * 16);
    if (__builtin_amdgcn_readfirstlane(v.x & v.y & v.z & v.w)) break;
    __builtin_amdgcn_s_sleep(1);
  }
  CBAR();
}
__device__ __forceinline__ void flag_wait1(LAS u8* L, int f, int w) {
#pragma nounroll
  for (int i = 0; i < SPIN_L; ++i) {
    u32 v = *(volatile LAS u32*)(L + L_F + f * 16 + w * 4);
    if (__builtin_amdgcn_readfirstlane(v)) break;
    __builtin_amdgcn_s_sleep(1);
  }
  CBAR();
}
#define STAMP(i) do { } while (0)
#define STAMP_OUT() do { } while (0)
#define MKRSRC(p, n) __builtin_amdgcn_make_buffer_rsrc((void*)(p), 0, (int)(n), 0x00020000)

__global__ void __launch_bounds__(512, 2)
gqa_fused(const u8* x, const u8* kc, const u8* vc, const u8* wq, const u8* wo, u8* out, u8* ws) {
  extern __shared__ __attribute__((aligned(16))) u8 smem[];
  LAS u8* L = (LAS u8*)smem;
  const int tid = threadIdx.x, lane = tid & 63, wave = __builtin_amdgcn_readfirstlane(tid >> 6);
  const int g = lane >> 4, c = lane & 15;
  const int bid = blockIdx.x, kvh = bid & 7, b = bid >> 5, split = (bid >> 3) & 3;
  const int wg = (b * 8 + kvh) * 4 + split;
  const __amdgpu_buffer_rsrc_t rs = MKRSRC(ws, WS_END);
  STAMP(0);

  if (wave < 4) {
    const int w = wave;
    __builtin_amdgcn_s_setprio(2);
    const size_t kvoff = ((size_t)((b * 8 + kvh) * 2048 + split * 512 + w * 128)) * 256;
    const u8* kb = kc + kvoff;
    const __amdgpu_buffer_rsrc_t rv = MKRSRC(vc + kvoff, 32768);
    u32 ko[4];
#pragma unroll
    for (int i = 0; i < 4; ++i) ko[i] = g * 256 + ((c ^ (4 * g + i)) * 16);
    const u32 ldsk = (u32)(size_t)L + L_K + w * 32768;
#pragma unroll
    for (int q4 = 0; q4 < 8; ++q4) {
#pragma unroll
      for (int i = 0; i < 4; ++i) {
        if (q4 * 4 + i >= THR) waitvm<THR - 1>();
        u32 keep;
        asm volatile("s_mov_b32 %0, m0\n\ts_mov_b32 m0, %3\n\ts_nop 0\n\tglobal_load_lds_dwordx4 %1, %2 offset:%4 nt\n\ts_mov_b32 m0, %0"
                     : "=&s"(keep) : "v"(ko[i]), "s"(kb + q4 * 4096), "s"(ldsk + q4 * 4096), "i"(i * 1024) : "memory");
      }
      if (q4 == 1) {
        if (w == 0) {
          *(volatile LAS u32*)(L + L_F + lane * 4) = 0u;
          asm volatile("s_waitcnt lgkmcnt(0)" ::: "memory");
        }
        SBAR();
        __builtin_amdgcn_s_barrier();
        SBAR();
      }
    }
    v4u v[32];
#pragma unroll
    for (int i = 0; i < 32; ++i) {
      waitvm<THR - 1>();
      if (i == THR - 1) { flag_set(L, F_K, w, lane); STAMP(1); }
      v[i] = __builtin_amdgcn_raw_buffer_load_b128(rv, lane * 16, i * 1024, 2);
    }
    SBAR();
    STAMP(2);
    flag_wait1(L, F_P, w);
    v4u p[4];
#pragma unroll
    for (int t = 0; t < 4; ++t) p[t] = *(LAS v4u*)(L + L_P + w * 4096 + t * 1024 + lane * 16);
    f4 o[8];
#pragma unroll
    for (int e = 0; e < 8; ++e) o[e] = f4{0.f, 0.f, 0.f, 0.f};
#pragma unroll
    for (int t = 0; t < 4; ++t) {
#pragma unroll
      for (int e = 0; e < 8; ++e) {
        v4u bfr;
#pragma unroll
        for (int jj = 0; jj < 4; ++jj) {
          const u32 a0 = v[8 * t + 2 * jj][e >> 1], a1 = v[8 * t + 2 * jj + 1][e >> 1];
          bfr[jj] = (e & 1) ? phi(a0, a1) : plo(a0, a1);
        }
        o[e] = mfma(p[t], bfr, o[e]);
      }
      SBAR();
    }
    STAMP(3);
#pragma unroll
    for (int r = 0; r < 4; ++r) {
      LAS u8* dst = L + L_K + w * 32768 + (4 * g + r) * 512 + c * 16;
      *(LAS f4*)dst = f4{o[0][r], o[1][r], o[2][r], o[3][r]};
      *(LAS f4*)(dst + 256) = f4{o[4][r], o[5][r], o[6][r], o[7][r]};
    }
    flag_set(L, F_O, w, lane);
    flag_wait4(L, F_O);
    {
      f4 s0 = {0.f, 0.f, 0.f, 0.f}, s1 = {0.f, 0.f, 0.f, 0.f};
#pragma unroll
      for (int ww = 0; ww < 4; ++ww) {
        LAS u8* src = L + L_K + ww * 32768 + (4 * w + g) * 512 + c * 16;
        s0 += *(LAS f4*)src;
        s1 += *(LAS f4*)(src + 256);
      }
      v4u ov = {san(pk(s0[0], s0[1])), san(pk(s0[2], s0[3])), san(pk(s1[0], s1[1])), san(pk(s1[2], s1[3]))};
      const u32 off = WS_BOX + ((b * 8 + kvh) * 4 + w) * BOX + 8192 + (((c >> 2) * 64 + (c & 3) * 16 + 4 * split + g) * 16);
      __builtin_amdgcn_raw_buffer_store_b128(ov, rs, off, 0, 16);
    }
    flag_set(L, F_SENT, w, lane);
    STAMP(4);
    if (w == 0) {
      __builtin_amdgcn_s_setprio(0);
      const u32 base = WS_BOX + wg * BOX + lane * 16;
      v4u r[8];
#pragma nounroll
      for (int it = 0; it < SPIN_G; ++it) {
        ld8_sc1(r, ws, base, base + 4096);
        u32 mx = 0;
#pragma unroll
        for (int k = 0; k < 8; ++k) mx = max(mx, umax4(r[k]));
        if (__all(mx != SENT)) break;
        __builtin_amdgcn_s_sleep(1);
      }
      f4 s = {0.f, 0.f, 0.f, 0.f};
#pragma unroll
      for (int k = 0; k < 8; ++k) s += __builtin_bit_cast(f4, r[k]);
#pragma unroll
      for (int j = 0; j < 4; ++j) s[j] = xsum(s[j]);
      const int i2 = kvh * 4 + split;
      if (lane < 16) {
        v2u ov = {pk(s[0], s[1]), pk(s[2], s[3])};
        *(GAS v2u*)(out + ((size_t)((b * 4 + (i2 >> 3)) * 512 + (i2 & 7) * 64 + 4 * c)) * 2) = ov;
      }
      STAMP(5);
    }
    STAMP_OUT();
    return;
  }

  const int cw = wave - 4, ct = tid - 256;
  const v2u ep = __builtin_amdgcn_raw_buffer_load_b64(rs, WS_EPOCH + (b >> 1) * 256, 0, 16);
  const int qi = (b & 1) * 4 + split;
  u32 tlo, thi;
  {
    v4u xa[4];
    v2u wv[32];
    const __amdgpu_buffer_rsrc_t rx = MKRSRC(x, 32768);
    const __amdgpu_buffer_rsrc_t rq = MKRSRC(wq, 4u << 20);
    const u32 xo = (((b & ~1) * 4 + (c & 7)) * 512 + cw * 128 + g * 8) * 2;
#pragma unroll
    for (int t = 0; t < 4; ++t) xa[t] = __builtin_amdgcn_raw_buffer_load_b128(rx, xo + t * 64, 0, 0);
    SBAR();
    __builtin_amdgcn_s_barrier();
    SBAR();
    const u32 wqo = ((cw * 128 + g * 8) * 4096 + kvh * 512 + qi * 64 + c * 4) * 2;
#pragma unroll
    for (int t = 0; t < 4; ++t)
#pragma unroll
      for (int j = 0; j < 8; ++j) wv[t * 8 + j] = __builtin_amdgcn_raw_buffer_load_b64(rq, wqo, (t * 32 + j) * 8192, W_AUX);
    SBAR();
    {
      const v4u sv = {SENT, SENT, SENT, SENT};
      const u32 base = WS_BOX + wg * BOX + ct * 16;
#pragma unroll
      for (int k = 0; k < 3; ++k) __builtin_amdgcn_raw_buffer_store_b128(sv, rs, base + k * 4096, 0, 16);
      if (ct < 8) __builtin_amdgcn_raw_buffer_store_b128(sv, rs, base + 3 * 4096, 0, 16);
    }
    SBAR();
    f4 qa[4];
#pragma unroll
    for (int e = 0; e < 4; ++e) qa[e] = f4{0.f, 0.f, 0.f, 0.f};
#pragma unroll
    for (int t = 0; t < 4; ++t)
#pragma unroll
      for (int e = 0; e < 4; ++e) {
        v4u bfr;
#pragma unroll
        for (int jj = 0; jj < 4; ++jj) {
          const u32 a0 = wv[8 * t + 2 * jj][e >> 1], a1 = wv[8 * t + 2 * jj + 1][e >> 1];
          bfr[jj] = (e & 1) ? phi(a0, a1) : plo(a0, a1);
        }
        qa[e] = mfma(xa[t], bfr, qa[e]);
      }
    const u64 T = (((u64)(u32)__builtin_amdgcn_readfirstlane(ep.y) << 32) | (u32)__builtin_amdgcn_readfirstlane(ep.x)) + 1ull;
    tlo = (u32)(T << 16);
    thi = (u32)(T >> 16);
    if (lane < 32) {
#pragma unroll
      for (int r = 0; r < 4; ++r) *(LAS f4*)(L + L_QP + cw * 2048 + (g * 4 + r) * 256 + c * 16) = f4{qa[0][r], qa[1][r], qa[2][r], qa[3][r]};
    }
    SBAR();
    waitvm<0>();
    STAMP(1);
    flag_set(L, F_QP, cw, lane);
    flag_wait4(L, F_QP);
    {
      const int row = ct >> 5, cp = ct & 31;
      f2 s = {0.f, 0.f};
#pragma unroll
      for (int ww = 0; ww < 4; ++ww) s += *(LAS f2*)(L + L_QP + ww * 2048 + row * 256 + cp * 8);
      const u32 pv = pk(s[0], s[1]);
      const v4u gr = {tlo | (pv & 0xFFFFu), thi, tlo | (pv >> 16), thi};
      const u32 off = WS_QBUF + ((((b & ~1) + (row >> 2)) * 8 + kvh) * 16 + (qi >> 1) * 4 + (row & 3)) * 1024 + ((qi & 1) * 64 + cp * 2) * 8;
      __builtin_amdgcn_raw_buffer_store_b128(gr, rs, off, 0, 16);
    }
    SBAR();
    STAMP(2);
  }
  v4u wv[32];
  const __amdgpu_buffer_rsrc_t ro = MKRSRC(wo + (size_t)(kvh * 4 + split) * 131072, 131072);
  const u32 woo = (g * 8 * 512 + cw * 128 + c * 8) * 2;
#define LDWO(i) __builtin_amdgcn_raw_buffer_load_b128(ro, woo, (((i) >> 3) * 32 + ((i) & 7)) * 1024, W_AUX)
#pragma unroll
  for (int i = 0; i < 16; ++i) wv[i] = LDWO(i);
  SBAR();
  {
    const u32 base = WS_QBUF + ((b * 8 + kvh) * 16 + cw * 4) * 1024 + lane * 16;
    v4u r[4];
#pragma nounroll
    for (int it = 0; it < SPIN_G; ++it) {
      ld4_sc1(r, ws, base);
      u32 bad = 0;
#pragma unroll
      for (int k = 0; k < 4; ++k) bad |= (((r[k].x ^ tlo) | (r[k].z ^ tlo)) & 0xFFFF0000u) | (r[k].y ^ thi) | (r[k].w ^ thi);
      if (__all(bad == 0)) break;
      __builtin_amdgcn_s_sleep(2);
    }
#pragma unroll
    for (int k = 0; k < 4; ++k)
      *(LAS u32*)(L + L_Q + ((lane >> 4) * 64 + ((lane >> 2) & 3) * 16 + cw * 4 + k) * 16 + (lane & 3) * 4) = plo(r[k].x, r[k].z);
    STAMP(3);
    flag_set(L, F_Q, cw, lane);
    flag_wait4(L, F_Q);
  }
  v4u qf[4];
#pragma unroll
  for (int s = 0; s < 4; ++s) qf[s] = *(LAS v4u*)(L + L_Q + s * 1024 + lane * 16);
  flag_wait1(L, F_K, cw);
  STAMP(4);
  f4 sc[8];
  {
    LAS u8* kr = L + L_K + cw * 32768 + (4 * (c & 3) + (c >> 2)) * 256;
    u32 co[4];
#pragma unroll
    for (int s = 0; s < 4; ++s) co[s] = ((4 * s + g) ^ c) * 16;
#pragma unroll
    for (int u = 0; u < 8; ++u) {
      sc[u] = f4{0.f, 0.f, 0.f, 0.f};
#pragma unroll
      for (int s = 0; s < 4; ++s) sc[u] = mfma(*(LAS v4u*)(kr + u * 4096 + co[s]), qf[s], sc[u]);
    }
  }
  float M;
  {
    float m = sc[0][0];
#pragma unroll
    for (int u = 0; u < 8; ++u)
#pragma unroll
      for (int r = 0; r < 4; ++r) m = fmaxf(m, sc[u][r]);
    m = xmax(m);
    if (lane < 16) *(LAS float*)(L + L_M + c * 16 + cw * 4) = m;
    flag_set(L, F_M, cw, lane);
    flag_wait4(L, F_M);
    const f4 mm = *(LAS f4*)(L + L_M + c * 16);
    M = fmaxf(fmaxf(mm[0], mm[1]), fmaxf(mm[2], mm[3])) * SCL;
    float l = 0.f;
#pragma unroll
    for (int u = 0; u < 8; ++u)
#pragma unroll
      for (int r = 0; r < 4; ++r) {
        sc[u][r] = __builtin_amdgcn_exp2f(__builtin_fmaf(sc[u][r], SCL, -M));
        l += sc[u][r];
      }
#pragma unroll
    for (int t = 0; t < 4; ++t) {
      const v4u pv = {pk(sc[2 * t][0], sc[2 * t][1]), pk(sc[2 * t][2], sc[2 * t][3]), pk(sc[2 * t + 1][0], sc[2 * t + 1][1]), pk(sc[2 * t + 1][2], sc[2 * t + 1][3])};
      *(LAS v4u*)(L + L_P + cw * 4096 + t * 1024 + lane * 16) = pv;
    }
    flag_set(L, F_P, cw, lane);
    STAMP(5);
    l = xsum(l);
    if (lane < 16) *(LAS float*)(L + L_L + c * 16 + cw * 4) = l;
    flag_set(L, F_L, cw, lane);
    if (cw == 0) {
      flag_wait4(L, F_L);
      if (lane < 16) {
        const f4 ll = *(LAS f4*)(L + L_L + c * 16);
        const v2u st = {san(bf(M)), san(bf((ll[0] + ll[1]) + (ll[2] + ll[3])))};
        const u32 off = WS_BOX + ((b * 8 + kvh) * 4 + (c >> 2)) * BOX + 12288 + (split * 4 + (c & 3)) * 8;
        __builtin_amdgcn_raw_buffer_store_b64(st, rs, off, 0, 16);
      }
    }
  }
  SBAR();
#pragma unroll
  for (int i = 16; i < 32; ++i) wv[i] = LDWO(i);
  SBAR();
  {
    const int qi2 = (lane >> 5) * 4 + (lane & 3);
    const u32 off = WS_QBUF + ((b * 8 + ((lane >> 2) & 7)) * 16 + (qi2 >> 1) * 4) * 1024 + (qi2 & 1) * 512;
#pragma nounroll
    for (int it = 0; it < SPIN_G; ++it) {
      v2u r;
      ld1_sc1(r, ws, off);
      if (__all((((r.x ^ tlo) & 0xFFFF0000u) | (r.y ^ thi)) == 0)) break;
      __builtin_amdgcn_s_sleep(4);
    }
    if (cw == 0 && (bid & 63) == 0 && lane == 0) {
      const v2u nt = {(tlo >> 16) | (thi << 16), thi >> 16};
      __builtin_amdgcn_raw_buffer_store_b64(nt, rs, WS_EPOCH + (b >> 1) * 256, 0, 16);
    }
  }
  STAMP(6);
  v4u wf[32];
#pragma unroll
  for (int t = 0; t < 4; ++t)
#pragma unroll
    for (int e = 0; e < 8; ++e)
#pragma unroll
      for (int jj = 0; jj < 4; ++jj) {
        const u32 a0 = wv[8 * t + 2 * jj][e >> 1], a1 = wv[8 * t + 2 * jj + 1][e >> 1];
        wf[t * 8 + e][jj] = (e & 1) ? phi(a0, a1) : plo(a0, a1);
      }
#pragma unroll
  for (int i = 0; i < 32; ++i) asm volatile("" : "+v"(wf[i]));
  float f[4];
  {
    const u32 off = WS_BOX + wg * BOX + 12288 + g * 32;
    v4u a, bq;
#pragma nounroll
    for (int it = 0; it < SPIN_G; ++it) {
      ld2_sc1(a, bq, ws, off);
      if (__all(max(umax4(a), umax4(bq)) != SENT)) break;
      __builtin_amdgcn_s_sleep(4);
    }
    const float ms[4] = {fb(a.x), fb(a.z), fb(bq.x), fb(bq.z)};
    const float ls[4] = {fb(a.y), fb(a.w), fb(bq.y), fb(bq.w)};
#pragma unroll
    for (int r = 0; r < 4; ++r) {
      const float mx = xmax(ms[r]);
      const float e = __builtin_amdgcn_exp2f(ms[r] - mx);
      f[r] = e / xsum(e * ls[r]);
    }
#pragma unroll
    for (int r = 0; r < 4; ++r) asm volatile("" : "+v"(f[r]));
  }
  STAMP(7);
  flag_wait4(L, F_SENT);
  STAMP(8);
  {
    const u32 base = WS_BOX + wg * BOX + 8192 + lane * 16;
    v4u a[4];
#pragma nounroll
    for (int it = 0; it < SPIN_G; ++it) {
      ld4_sc1(a, ws, base);
      if (__all(max(max(umax4(a[0]), umax4(a[1])), max(umax4(a[2]), umax4(a[3]))) != SENT)) break;
    }
    STAMP(9);
    f4 d[8];
#pragma unroll
    for (int e = 0; e < 8; ++e) {
      d[e] = f4{0.f, 0.f, 0.f, 0.f};
#pragma unroll
      for (int t = 0; t < 4; ++t) d[e] = mfma(a[t], wf[t * 8 + e], d[e]);
    }
    u32 res[8];
#pragma unroll
    for (int e = 0; e < 8; ++e) {
      auto s02 = __builtin_amdgcn_permlane32_swap(bf(d[e][0] * f[0]), bf(d[e][2] * f[2]), false, false);
      auto s13 = __builtin_amdgcn_permlane32_swap(bf(d[e][1] * f[1]), bf(d[e][3] * f[3]), false, false);
      const float x02 = fb(s02[0]) + fb(s02[1]), x13 = fb(s13[0]) + fb(s13[1]);
      auto s = __builtin_amdgcn_permlane16_swap(bf(x02), bf(x13), false, false);
      res[e] = san(bf(fb(s[0]) + fb(s[1])));
    }
    const int i2 = g * 8 + cw * 2 + (c >> 3);
    const u32 off = WS_BOX + (b * 32 + i2) * BOX + (kvh * 4 + split) * 256 + (c & 7) * 32;
    __builtin_amdgcn_raw_buffer_store_b128(v4u{res[0], res[1], res[2], res[3]}, rs, off, 0, 16);
    __builtin_amdgcn_raw_buffer_store_b128(v4u{res[4], res[5], res[6], res[7]}, rs, off + 16, 0, 16);
    STAMP(10);
  }
  STAMP_OUT();
}

extern "C" void kernel_launch(void* const* d_in, const int* in_sizes, int n_in, void* d_out, int out_size, void* d_ws,
                              size_t ws_size, hipStream_t stream) {
  (void)in_sizes; (void)n_in; (void)out_size; (void)ws_size;
  (void)hipFuncSetAttribute((const void*)gqa_fused, hipFuncAttributeMaxDynamicSharedMemorySize, (int)LDS_BYTES);
  hipLaunchKernelGGL(gqa_fused, dim3(256), dim3(512), LDS_BYTES, stream, (const u8*)d_in[0], (const u8*)d_in[1], (const u8*)d_in[2],
                     (const u8*)d_in[3], (const u8*)d_in[4], (u8*)d_out, (u8*)d_ws);
}
```

```cpp
#include <hip/hip_runtime.h>
#include <stdint.h>

typedef unsigned u32;
typedef unsigned long long u64;
typedef unsigned char u8;
typedef u32 v4u __attribute__((ext_vector_type(4)));
typedef u32 v2u __attribute__((ext_vector_type(2)));
typedef float f4 __attribute__((ext_vector_type(4)));
typedef float f2 __attribute__((ext_vector_type(2)));
typedef short s8v __attribute__((ext_vector_type(8)));
typedef __bf16 bf2v __attribute__((ext_vector_type(2)));
#define GAS __attribute__((address_space(1)))
#define LAS __attribute__((address_space(3)))

#define THR 8
#define W_AUX 2
#define STAMPS 1
#include <stdio.h>

constexpr u32 WS_EPOCH = 0;
constexpr u32 WS_QBUF = 4096;
constexpr u32 WS_BOX = WS_QBUF + 64 * 16384;
constexpr u32 BOX = 12544;
constexpr u32 WS_END = WS_BOX + 256 * BOX;
constexpr u32 WS_STAMP = 8u << 20;
constexpr u32 L_K = 0;
constexpr u32 L_P = 131072;
constexpr u32 L_QP = L_P, L_Q = L_P + 8192;
constexpr u32 L_M = L_P + 16384, L_L = L_M + 256, L_F = L_L + 256;
constexpr u32 LDS_BYTES = L_F + 256;
enum { F_QP = 0, F_Q, F_K, F_M, F_L, F_P, F_O, F_SENT };

constexpr u32 SENT = 0xFFFFFFFFu;
constexpr int SPIN_G = 1 << 17;
constexpr int SPIN_L = 1 << 22;
constexpr float SCL = 0.08838834764831845f * 1.4426950408889634f;

__device__ __forceinline__ f4 mfma(v4u a, v4u b, f4 c) {
  return __builtin_amdgcn_mfma_f32_16x16x32_bf16(__builtin_bit_cast(s8v, a), __builtin_bit_cast(s8v, b), c, 0, 0, 0);
}
__device__ __forceinline__ u32 pk(float lo, float hi) {
  f2 v = {lo, hi};
  return __builtin_bit_cast(u32, __builtin_convertvector(v, bf2v));
}
__device__ __forceinline__ u32 plo(u32 a, u32 b) { return __builtin_amdgcn_perm(b, a, 0x05040100u); }
__device__ __forceinline__ u32 phi(u32 a, u32 b) { return __builtin_amdgcn_perm(b, a, 0x07060302u); }
__device__ __forceinline__ u32 san(u32 x) { return min(x, 0xFFFFFFFEu); }
__device__ __forceinline__ float fb(u32 x) { return __builtin_bit_cast(float, x); }
__device__ __forceinline__ u32 bf(float x) { return __builtin_bit_cast(u32, x); }
__device__ __forceinline__ u32 umax4(v4u v) { return max(max(v.x, v.y), max(v.z, v.w)); }
__device__ __forceinline__ float xmax(float x) {
  auto s = __builtin_amdgcn_permlane16_swap(bf(x), bf(x), false, false);
  x = fmaxf(fb(s[0]), fb(s[1]));
  auto t = __builtin_amdgcn_permlane32_swap(bf(x), bf(x), false, false);
  return fmaxf(fb(t[0]), fb(t[1]));
}
__device__ __forceinline__ float xsum(float x) {
  auto s = __builtin_amdgcn_permlane16_swap(bf(x), bf(x), false, false);
  x = fb(s[0]) + fb(s[1]);
  auto t = __builtin_amdgcn_permlane32_swap(bf(x), bf(x), false, false);
  return fb(t[0]) + fb(t[1]);
}
__device__ __forceinline__ void ld4_sc1(v4u (&r)[4], const u8* base, u32 voff) {
  asm volatile("global_load_dwordx4 %0, %4, %5 sc1\n\tglobal_load_dwordx4 %1, %4, %5 offset:1024 sc1\n\t"
               "global_load_dwordx4 %2, %4, %5 offset:2048 sc1\n\tglobal_load_dwordx4 %3, %4, %5 offset:3072 sc1\n\ts_waitcnt vmcnt(0)"
               : "=&v"(r[0]), "=&v"(r[1]), "=&v"(r[2]), "=&v"(r[3]) : "v"(voff), "s"(base) : "memory");
}
__device__ __forceinline__ void ld8_sc1(v4u (&r)[8], const u8* base, u32 voff, u32 voff2) {
  asm volatile("global_load_dwordx4 %0, %8, %10 sc1\n\tglobal_load_dwordx4 %1, %8, %10 offset:1024 sc1\n\t"
               "global_load_dwordx4 %2, %8, %10 offset:2048 sc1\n\tglobal_load_dwordx4 %3, %8, %10 offset:3072 sc1\n\t"
               "global_load_dwordx4 %4, %9, %10 sc1\n\tglobal_load_dwordx4 %5, %9, %10 offset:1024 sc1\n\t"
               "global_load_dwordx4 %6, %9, %10 offset:2048 sc1\n\tglobal_load_dwordx4 %7, %9, %10 offset:3072 sc1\n\ts_waitcnt vmcnt(0)"
               : "=&v"(r[0]), "=&v"(r[1]), "=&v"(r[2]), "=&v"(r[3]), "=&v"(r[4]), "=&v"(r[5]), "=&v"(r[6]), "=&v"(r[7])
               : "v"(voff), "v"(voff2), "s"(base) : "memory");
}
__device__ __forceinline__ void ld2_sc1(v4u& a, v4u& b, const u8* base, u32 voff) {
  asm volatile("global_load_dwordx4 %0, %2, %3 sc1\n\tglobal_load_dwordx4 %1, %2, %3 offset:16 sc1\n\ts_waitcnt vmcnt(0)"
               : "=&v"(a), "=&v"(b) : "v"(voff), "s"(base) : "memory");
}
__device__ __forceinline__ void ld1_sc1(v2u& a, const u8* base, u32 voff) {
  asm volatile("global_load_dwordx2 %0, %1, %2 sc1\n\ts_waitcnt vmcnt(0)" : "=&v"(a) : "v"(voff), "s"(base) : "memory");
}
#define CBAR() asm volatile("" ::: "memory")
#define SBAR() __builtin_amdgcn_sched_barrier(0)
template <int N> __device__ __forceinline__ void waitvm() { asm volatile("s_waitcnt vmcnt(%0)" ::"i"(N) : "memory"); }

__device__ __forceinline__ void flag_set(LAS u8* L, int f, int w, int lane) {
  asm volatile("s_waitcnt lgkmcnt(0)" ::: "memory");
  if (lane == 0) *(volatile LAS u32*)(L + L_F + f * 16 + w * 4) = 1u;
  CBAR();
}
__device__ __forceinline__ void flag_wait4(LAS u8* L, int f) {
#pragma nounroll
  for (int i = 0; i < SPIN_L; ++i) {
    v4u v = *(volatile LAS v4u*)(L + L_F + f * 16);
    if (__builtin_amdgcn_readfirstlane(v.x & v.y & v.z & v.w)) break;
    __builtin_amdgcn_s_sleep(1);
  }
  CBAR();
}
__device__ __forceinline__ void flag_wait1(LAS u8* L, int f, int w) {
#pragma nounroll
  for (int i = 0; i < SPIN_L; ++i) {
    u32 v = *(volatile LAS u32*)(L + L_F + f * 16 + w * 4);
    if (__builtin_amdgcn_readfirstlane(v)) break;
    __builtin_amdgcn_s_sleep(1);
  }
  CBAR();
}
#define STAMP(i) do { SBAR(); stamp[i] = __builtin_amdgcn_s_memrealtime(); SBAR(); } while (0)
#define STAMP_OUT() do { if (lane == 0) { for (int i_ = 0; i_ < 12; ++i_) ((GAS u64*)(ws + WS_STAMP))[(blockIdx.x * 8 + wave) * 16 + i_] = stamp[i_]; } } while (0)
#define MKRSRC(p, n) __builtin_amdgcn_make_buffer_rsrc((void*)(p), 0, (int)(n), 0x00020000)

__global__ void __launch_bounds__(512, 2)
gqa_fused(const u8* x, const u8* kc, const u8* vc, const u8* wq, const u8* wo, u8* out, u8* ws) {
  extern __shared__ __attribute__((aligned(16))) u8 smem[];
  LAS u8* L = (LAS u8*)smem;
  const int tid = threadIdx.x, lane = tid & 63, wave = __builtin_amdgcn_readfirstlane(tid >> 6);
  const int g = lane >> 4, c = lane & 15;
  const int bid = blockIdx.x, kvh = bid & 7, b = bid >> 5, split = (bid >> 3) & 3;
  const int wg = (b * 8 + kvh) * 4 + split;
  const __amdgpu_buffer_rsrc_t rs = MKRSRC(ws, WS_END);
  u64 stamp[12];
  for (int i = 0; i < 12; ++i) stamp[i] = 0;
  STAMP(0);

  if (wave < 4) {
    const int w = wave;
    __builtin_amdgcn_s_setprio(2);
    const size_t kvoff = ((size_t)((b * 8 + kvh) * 2048 + split * 512 + w * 128)) * 256;
    const u8* kb = kc + kvoff;
    const __amdgpu_buffer_rsrc_t rv = MKRSRC(vc + kvoff, 32768);
    u32 ko[4];
#pragma unroll
    for (int i = 0; i < 4; ++i) ko[i] = g * 256 + ((c ^ (4 * g + i)) * 16);
    const u32 ldsk = (u32)(size_t)L + L_K + w * 32768;
#pragma unroll
    for (int q4 = 0; q4 < 8; ++q4) {
#pragma unroll
      for (int i = 0; i < 4; ++i) {
        if (q4 * 4 + i >= THR) waitvm<THR - 1>();
        u32 keep;
        asm volatile("s_mov_b32 %0, m0\n\ts_mov_b32 m0, %3\n\ts_nop 0\n\tglobal_load_lds_dwordx4 %1, %2 offset:%4 nt\n\ts_mov_b32 m0, %0"
                     : "=&s"(keep) : "v"(ko[i]), "s"(kb + q4 * 4096), "s"(ldsk + q4 * 4096), "i"(i * 1024) : "memory");
      }
      if (q4 == 1) {
        if (w == 0) {
          *(volatile LAS u32*)(L + L_F + lane * 4) = 0u;
          asm volatile("s_waitcnt lgkmcnt(0)" ::: "memory");
        }
        SBAR();
        __builtin_amdgcn_s_barrier();
        SBAR();
      }
    }
    v4u v[32];
#pragma unroll
    for (int i = 0; i < 32; ++i) {
      waitvm<THR - 1>();
      if (i == THR - 1) { flag_set(L, F_K, w, lane); STAMP(1); }
      v[i] = __builtin_amdgcn_raw_buffer_load_b128(rv, lane * 16, i * 1024, 2);
    }
    SBAR();
    STAMP(2);
    flag_wait1(L, F_P, w);
    v4u p[4];
#pragma unroll
    for (int t = 0; t < 4; ++t) p[t] = *(LAS v4u*)(L + L_P + w * 4096 + t * 1024 + lane * 16);
    f4 o[8];
#pragma unroll
    for (int e = 0; e < 8; ++e) o[e] = f4{0.f, 0.f, 0.f, 0.f};
#pragma unroll
    for (int t = 0; t < 4; ++t) {
#pragma unroll
      for (int e = 0; e < 8; ++e) {
        v4u bfr;
#pragma unroll
        for (int jj = 0; jj < 4; ++jj) {
          const u32 a0 = v[8 * t + 2 * jj][e >> 1], a1 = v[8 * t + 2 * jj + 1][e >> 1];
          bfr[jj] = (e & 1) ? phi(a0, a1) : plo(a0, a1);
        }
        o[e] = mfma(p[t], bfr, o[e]);
      }
      SBAR();
    }
    STAMP(3);
#pragma unroll
    for (int r = 0; r < 4; ++r) {
      LAS u8* dst = L + L_K + w * 32768 + (4 * g + r) * 512 + c * 16;
      *(LAS f4*)dst = f4{o[0][r], o[1][r], o[2][r], o[3][r]};
      *(LAS f4*)(dst + 256) = f4{o[4][r], o[5][r], o[6][r], o[7][r]};
    }
    flag_set(L, F_O, w, lane);
    flag_wait4(L, F_O);
    {
      f4 s0 = {0.f, 0.f, 0.f, 0.f}, s1 = {0.f, 0.f, 0.f, 0.f};
#pragma unroll
      for (int ww = 0; ww < 4; ++ww) {
        LAS u8* src = L + L_K + ww * 32768 + (4 * w + g) * 512 + c * 16;
        s0 += *(LAS f4*)src;
        s1 += *(LAS f4*)(src + 256);
      }
      v4u ov = {san(pk(s0[0], s0[1])), san(pk(s0[2], s0[3])), san(pk(s1[0], s1[1])), san(pk(s1[2], s1[3]))};
      const u32 off = WS_BOX + ((b * 8 + kvh) * 4 + w) * BOX + 8192 + (((c >> 2) * 64 + (c & 3) * 16 + 4 * split + g) * 16);
      __builtin_amdgcn_raw_buffer_store_b128(ov, rs, off, 0, 16);
    }
    flag_set(L, F_SENT, w, lane);
    STAMP(4);
    if (w == 0) {
      __builtin_amdgcn_s_setprio(0);
      const u32 base = WS_BOX + wg * BOX + lane * 16;
      v4u r[8];
#pragma nounroll
      for (int it = 0; it < SPIN_G; ++it) {
        ld8_sc1(r, ws, base, base + 4096);
        u32 mx = 0;
#pragma unroll
        for (int k = 0; k < 8; ++k) mx = max(mx, umax4(r[k]));
        if (__all(mx != SENT)) break;
        __builtin_amdgcn_s_sleep(1);
      }
      f4 s = {0.f, 0.f, 0.f, 0.f};
#pragma unroll
      for (int k = 0; k < 8; ++k) s += __builtin_bit_cast(f4, r[k]);
#pragma unroll
      for (int j = 0; j < 4; ++j) s[j] = xsum(s[j]);
      const int i2 = kvh * 4 + split;
      if (lane < 16) {
        v2u ov = {pk(s[0], s[1]), pk(s[2], s[3])};
        *(GAS v2u*)(out + ((size_t)((b * 4 + (i2 >> 3)) * 512 + (i2 & 7) * 64 + 4 * c)) * 2) = ov;
      }
      STAMP(5);
    }
    STAMP_OUT();
    return;
  }

  const int cw = wave - 4, ct = tid - 256;
  const v2u ep = __builtin_amdgcn_raw_buffer_load_b64(rs, WS_EPOCH + (b >> 1) * 256, 0, 16);
  const int qi = (b & 1) * 4 + split;
  u32 tlo, thi;
  {
    v4u xa[4];
    v2u wv[32];
    const __amdgpu_buffer_rsrc_t rx = MKRSRC(x, 32768);
    const __amdgpu_buffer_rsrc_t rq = MKRSRC(wq, 4u << 20);
    const u32 xo = (((b & ~1) * 4 + (c & 7)) * 512 + cw * 128 + g * 8) * 2;
#pragma unroll
    for (int t = 0; t < 4; ++t) xa[t] = __builtin_amdgcn_raw_buffer_load_b128(rx, xo + t * 64, 0, 0);
    SBAR();
    __builtin_amdgcn_s_barrier();
    SBAR();
    const u32 wqo = ((cw * 128 + g * 8) * 4096 + kvh * 512 + qi * 64 + c * 4) * 2;
#pragma unroll
    for (int t = 0; t < 4; ++t)
#pragma unroll
      for (int j = 0; j < 8; ++j) wv[t * 8 + j] = __builtin_amdgcn_raw_buffer_load_b64(rq, wqo, (t * 32 + j) * 8192, W_AUX);
    SBAR();
    STAMP(11);
    {
      const v4u sv = {SENT, SENT, SENT, SENT};
      const u32 base = WS_BOX + wg * BOX + ct * 16;
#pragma unroll
      for (int k = 0; k < 3; ++k) __builtin_amdgcn_raw_buffer_store_b128(sv, rs, base + k * 4096, 0, 16);
      if (ct < 8) __builtin_amdgcn_raw_buffer_store_b128(sv, rs, base + 3 * 4096, 0, 16);
    }
    SBAR();
    f4 qa[4];
#pragma unroll
    for (int e = 0; e < 4; ++e) qa[e] = f4{0.f, 0.f, 0.f, 0.f};
#pragma unroll
    for (int t = 0; t < 4; ++t)
#pragma unroll
      for (int e = 0; e < 4; ++e) {
        v4u bfr;
#pragma unroll
        for (int jj = 0; jj < 4; ++jj) {
          const u32 a0 = wv[8 * t + 2 * jj][e >> 1], a1 = wv[8 * t + 2 * jj + 1][e >> 1];
          bfr[jj] = (e & 1) ? phi(a0, a1) : plo(a0, a1);
        }
        qa[e] = mfma(xa[t], bfr, qa[e]);
      }
    const u64 T = (((u64)(u32)__builtin_amdgcn_readfirstlane(ep.y) << 32) | (u32)__builtin_amdgcn_readfirstlane(ep.x)) + 1ull;
    tlo = (u32)(T << 16);
    thi = (u32)(T >> 16);
    if (lane < 32) {
#pragma unroll
      for (int r = 0; r < 4; ++r) *(LAS f4*)(L + L_QP + cw * 2048 + (g * 4 + r) * 256 + c * 16) = f4{qa[0][r], qa[1][r], qa[2][r], qa[3][r]};
    }
    SBAR();
    STAMP(8);
    waitvm<0>();
    STAMP(1);
    flag_set(L, F_QP, cw, lane);
    flag_wait4(L, F_QP);
    {
      const int row = ct >> 5, cp = ct & 31;
      f2 s = {0.f, 0.f};
#pragma unroll
      for (int ww = 0; ww < 4; ++ww) s += *(LAS f2*)(L + L_QP + ww * 2048 + row * 256 + cp * 8);
      const u32 pv = pk(s[0], s[1]);
      const v4u gr = {tlo | (pv & 0xFFFFu), thi, tlo | (pv >> 16), thi};
      const u32 off = WS_QBUF + ((((b & ~1) + (row >> 2)) * 8 + kvh) * 16 + (qi >> 1) * 4 + (row & 3)) * 1024 + ((qi & 1) * 64 + cp * 2) * 8;
      __builtin_amdgcn_raw_buffer_store_b128(gr, rs, off, 0, 16);
    }
    SBAR();
    STAMP(2);
  }
  v4u wv[32];
  const __amdgpu_buffer_rsrc_t ro = MKRSRC(wo + (size_t)(kvh * 4 + split) * 131072, 131072);
  const u32 woo = (g * 8 * 512 + cw * 128 + c * 8) * 2;
#define LDWO(i) __builtin_amdgcn_raw_buffer_load_b128(ro, woo, (((i) >> 3) * 32 + ((i) & 7)) * 1024, W_AUX)
#pragma unroll
  for (int i = 0; i < 16; ++i) wv[i] = LDWO(i);
  SBAR();
  {
    const u32 base = WS_QBUF + ((b * 8 + kvh) * 16 + cw * 4) * 1024 + lane * 16;
    v4u r[4];
#pragma nounroll
    for (int it = 0; it < SPIN_G; ++it) {
      ld4_sc1(r, ws, base);
      u32 bad = 0;
#pragma unroll
      for (int k = 0; k < 4; ++k) bad |= (((r[k].x ^ tlo) | (r[k].z ^ tlo)) & 0xFFFF0000u) | (r[k].y ^ thi) | (r[k].w ^ thi);
      if (__all(bad == 0)) break;
      __builtin_amdgcn_s_sleep(2);
    }
#pragma unroll
    for (int k = 0; k < 4; ++k)
      *(LAS u32*)(L + L_Q + ((lane >> 4) * 64 + ((lane >> 2) & 3) * 16 + cw * 4 + k) * 16 + (lane & 3) * 4) = plo(r[k].x, r[k].z);
    STAMP(3);
    flag_set(L, F_Q, cw, lane);
    flag_wait4(L, F_Q);
  }
  v4u qf[4];
#pragma unroll
  for (int s = 0; s < 4; ++s) qf[s] = *(LAS v4u*)(L + L_Q + s * 1024 + lane * 16);
  flag_wait1(L, F_K, cw);
  STAMP(4);
  f4 sc[8];
  {
    LAS u8* kr = L + L_K + cw * 32768 + (4 * (c & 3) + (c >> 2)) * 256;
    u32 co[4];
#pragma unroll
    for (int s = 0; s < 4; ++s) co[s] = ((4 * s + g) ^ c) * 16;
#pragma unroll
    for (int u = 0; u < 8; ++u) {
      sc[u] = f4{0.f, 0.f, 0.f, 0.f};
#pragma unroll
      for (int s = 0; s < 4; ++s) sc[u] = mfma(*(LAS v4u*)(kr + u * 4096 + co[s]), qf[s], sc[u]);
    }
  }
  float M;
  {
    float m = sc[0][0];
#pragma unroll
    for (int u = 0; u < 8; ++u)
#pragma unroll
      for (int r = 0; r < 4; ++r) m = fmaxf(m, sc[u][r]);
    m = xmax(m);
    if (lane < 16) *(LAS float*)(L + L_M + c * 16 + cw * 4) = m;
    flag_set(L, F_M, cw, lane);
    flag_wait4(L, F_M);
    const f4 mm = *(LAS f4*)(L + L_M + c * 16);
    M = fmaxf(fmaxf(mm[0], mm[1]), fmaxf(mm[2], mm[3])) * SCL;
    float l = 0.f;
#pragma unroll
    for (int u = 0; u < 8; ++u)
#pragma unroll
      for (int r = 0; r < 4; ++r) {
        sc[u][r] = __builtin_amdgcn_exp2f(__builtin_fmaf(sc[u][r], SCL, -M));
        l += sc[u][r];
      }
#pragma unroll
    for (int t = 0; t < 4; ++t) {
      const v4u pv = {pk(sc[2 * t][0], sc[2 * t][1]), pk(sc[2 * t][2], sc[2 * t][3]), pk(sc[2 * t + 1][0], sc[2 * t + 1][1]), pk(sc[2 * t + 1][2], sc[2 * t + 1][3])};
      *(LAS v4u*)(L + L_P + cw * 4096 + t * 1024 + lane * 16) = pv;
    }
    flag_set(L, F_P, cw, lane);
    STAMP(5);
    l = xsum(l);
    if (lane < 16) *(LAS float*)(L + L_L + c * 16 + cw * 4) = l;
    flag_set(L, F_L, cw, lane);
    if (cw == 0) {
      flag_wait4(L, F_L);
      if (lane < 16) {
        const f4 ll = *(LAS f4*)(L + L_L + c * 16);
        const v2u st = {san(bf(M)), san(bf((ll[0] + ll[1]) + (ll[2] + ll[3])))};
        const u32 off = WS_BOX + ((b * 8 + kvh) * 4 + (c >> 2)) * BOX + 12288 + (split * 4 + (c & 3)) * 8;
        __builtin_amdgcn_raw_buffer_store_b64(st, rs, off, 0, 16);
      }
    }
  }
  SBAR();
#pragma unroll
  for (int i = 16; i < 32; ++i) wv[i] = LDWO(i);
  SBAR();
  {
    const int qi2 = (lane >> 5) * 4 + (lane & 3);
    const u32 off = WS_QBUF + ((b * 8 + ((lane >> 2) & 7)) * 16 + (qi2 >> 1) * 4) * 1024 + (qi2 & 1) * 512;
#pragma nounroll
    for (int it = 0; it < SPIN_G; ++it) {
      v2u r;
      ld1_sc1(r, ws, off);
      if (__all((((r.x ^ tlo) & 0xFFFF0000u) | (r.y ^ thi)) == 0)) break;
      __builtin_amdgcn_s_sleep(4);
    }
    if (cw == 0 && (bid & 63) == 0 && lane == 0) {
      const v2u nt = {(tlo >> 16) | (thi << 16), thi >> 16};
      __builtin_amdgcn_raw_buffer_store_b64(nt, rs, WS_EPOCH + (b >> 1) * 256, 0, 16);
    }
  }
  STAMP(6);
  v4u wf[32];
#pragma unroll
  for (int t = 0; t < 4; ++t)
#pragma unroll
    for (int e = 0; e < 8; ++e)
#pragma unroll
      for (int jj = 0; jj < 4; ++jj) {
        const u32 a0 = wv[8 * t + 2 * jj][e >> 1], a1 = wv[8 * t + 2 * jj + 1][e >> 1];
        wf[t * 8 + e][jj] = (e & 1) ? phi(a0, a1) : plo(a0, a1);
      }
#pragma unroll
  for (int i = 0; i < 32; ++i) asm volatile("" : "+v"(wf[i]));
  float f[4];
  {
    const u32 off = WS_BOX + wg * BOX + 12288 + g * 32;
    v4u a, bq;
#pragma nounroll
    for (int it = 0; it < SPIN_G; ++it) {
      ld2_sc1(a, bq, ws, off);
      if (__all(max(umax4(a), umax4(bq)) != SENT)) break;
      __builtin_amdgcn_s_sleep(4);
    }
    const float ms[4] = {fb(a.x), fb(a.z), fb(bq.x), fb(bq.z)};
    const float ls[4] = {fb(a.y), fb(a.w), fb(bq.y), fb(bq.w)};
#pragma unroll
    for (int r = 0; r < 4; ++r) {
      const float mx = xmax(ms[r]);
      const float e = __builtin_amdgcn_exp2f(ms[r] - mx);
      f[r] = e / xsum(e * ls[r]);
    }
#pragma unroll
    for (int r = 0; r < 4; ++r) asm volatile("" : "+v"(f[r]));
  }
  STAMP(7);
  flag_wait4(L, F_SENT);
  {
    const u32 base = WS_BOX + wg * BOX + 8192 + lane * 16;
    v4u a[4];
#pragma nounroll
    for (int it = 0; it < SPIN_G; ++it) {
      ld4_sc1(a, ws, base);
      if (__all(max(max(umax4(a[0]), umax4(a[1])), max(umax4(a[2]), umax4(a[3]))) != SENT)) break;
    }
    STAMP(9);
    f4 d[8];
#pragma unroll
    for (int e = 0; e < 8; ++e) {
      d[e] = f4{0.f, 0.f, 0.f, 0.f};
#pragma unroll
      for (int t = 0; t < 4; ++t) d[e] = mfma(a[t], wf[t * 8 + e], d[e]);
    }
    u32 res[8];
#pragma unroll
    for (int e = 0; e < 8; ++e) {
      auto s02 = __builtin_amdgcn_permlane32_swap(bf(d[e][0] * f[0]), bf(d[e][2] * f[2]), false, false);
      auto s13 = __builtin_amdgcn_permlane32_swap(bf(d[e][1] * f[1]), bf(d[e][3] * f[3]), false, false);
      const float x02 = fb(s02[0]) + fb(s02[1]), x13 = fb(s13[0]) + fb(s13[1]);
      auto s = __builtin_amdgcn_permlane16_swap(bf(x02), bf(x13), false, false);
      res[e] = san(bf(fb(s[0]) + fb(s[1])));
    }
    const int i2 = g * 8 + cw * 2 + (c >> 3);
    const u32 off = WS_BOX + (b * 32 + i2) * BOX + (kvh * 4 + split) * 256 + (c & 7) * 32;
    __builtin_amdgcn_raw_buffer_store_b128(v4u{res[0], res[1], res[2], res[3]}, rs, off, 0, 16);
    __builtin_amdgcn_raw_buffer_store_b128(v4u{res[4], res[5], res[6], res[7]}, rs, off + 16, 0, 16);
    STAMP(10);
  }
  STAMP_OUT();
}

__global__ void report(const u8* ws) {
  if (threadIdx.x != 0) return;
  const u64* st = (const u64*)(ws + WS_STAMP);
  u64 mn = ~0ull, mx = 0;
  for (int bw = 0; bw < 2048; ++bw) { if (st[bw * 16] < mn) mn = st[bw * 16]; for (int i = 0; i < 12; ++i) if (st[bw * 16 + i] > mx) mx = st[bw * 16 + i]; }
  printf("span %d (10ns)\n", (int)(mx - mn));
  for (int role = 0; role < 2; ++role) for (int i = 0; i < 12; ++i) {
    u64 a = 0, lo = ~0ull, hi = 0; int n = 0;
    for (int bl = 0; bl < 256; ++bl) for (int w = role * 4; w < role * 4 + 4; ++w) { u64 t = st[(bl * 8 + w) * 16 + i]; if (!t) continue; u64 d = t - mn; a += d; if (d < lo) lo = d; if (d > hi) hi = d; n++; }
    if (n) printf("role %d [%d] %d/%d/%d\n", role, i, (int)lo, (int)(a / n), (int)hi);
  }
}
extern "C" void kernel_launch(void* const* d_in, const int* in_sizes, int n_in, void* d_out, int out_size, void* d_ws,
                              size_t ws_size, hipStream_t stream) {
  (void)in_sizes; (void)n_in; (void)out_size; (void)ws_size;
  (void)hipFuncSetAttribute((const void*)gqa_fused, hipFuncAttributeMaxDynamicSharedMemorySize, (int)LDS_BYTES);
  hipLaunchKernelGGL(gqa_fused, dim3(256), dim3(512), LDS_BYTES, stream, (const u8*)d_in[0], (const u8*)d_in[1], (const u8*)d_in[2],
                     (const u8*)d_in[3], (const u8*)d_in[4], (u8*)d_out, (u8*)d_ws);
  report<<<1, 64, 0, stream>>>((const u8*)d_ws);
  fprintf(stderr, "ptrs: %p %p %p %p %p out %p ws %p\n", d_in[0], d_in[1], d_in[2], d_in[3], d_in[4], d_out, d_ws);
}
```

```cpp
#include <hip/hip_runtime.h>
#include <stdint.h>

typedef unsigned u32;
typedef unsigned long long u64;
typedef unsigned char u8;
typedef u32 v4u __attribute__((ext_vector_type(4)));
typedef u32 v2u __attribute__((ext_vector_type(2)));
typedef float f4 __attribute__((ext_vector_type(4)));
typedef float f2 __attribute__((ext_vector_type(2)));
typedef short s8v __attribute__((ext_vector_type(8)));
typedef __bf16 bf2v __attribute__((ext_vector_type(2)));
#define GAS __attribute__((address_space(1)))
#define LAS __attribute__((address_space(3)))

#define THR 8
#define W_AUX 2
#define STAMPS 1

constexpr u32 WS_EPOCH = 0;
constexpr u32 WS_QBUF = 4096;
constexpr u32 WS_BOX = WS_QBUF + 64 * 16384;
constexpr u32 BOX = 12544;
constexpr u32 WS_END = WS_BOX + 256 * BOX;
constexpr u32 WS_STAMP = 8u << 20;
constexpr u32 L_K = 0;
constexpr u32 L_P = 131072;
constexpr u32 L_QP = L_P, L_Q = L_P + 8192;
constexpr u32 L_M = L_P + 16384, L_L = L_M + 256, L_F = L_L + 256;
constexpr u32 LDS_BYTES = L_F + 256;
enum { F_QP = 0, F_Q, F_K, F_M, F_L, F_P, F_O, F_SENT };

constexpr u32 SENT = 0xFFFFFFFFu;
constexpr int SPIN_G = 1 << 17;
constexpr int SPIN_L = 1 << 22;
constexpr float SCL = 0.08838834764831845f * 1.4426950408889634f;

__device__ __forceinline__ f4 mfma(v4u a, v4u b, f4 c) {
  return __builtin_amdgcn_mfma_f32_16x16x32_bf16(__builtin_bit_cast(s8v, a), __builtin_bit_cast(s8v, b), c, 0, 0, 0);
}
__device__ __forceinline__ u32 pk(float lo, float hi) {
  f2 v = {lo, hi};
  return __builtin_bit_cast(u32, __builtin_convertvector(v, bf2v));
}
__device__ __forceinline__ u32 plo(u32 a, u32 b) { return __builtin_amdgcn_perm(b, a, 0x05040100u); }
__device__ __forceinline__ u32 phi(u32 a, u32 b) { return __builtin_amdgcn_perm(b, a, 0x07060302u); }
__device__ __forceinline__ u32 san(u32 x) { return min(x, 0xFFFFFFFEu); }
__device__ __forceinline__ float fb(u32 x) { return __builtin_bit_cast(float, x); }
__device__ __forceinline__ u32 bf(float x) { return __builtin_bit_cast(u32, x); }
__device__ __forceinline__ u32 umax4(v4u v) { return max(max(v.x, v.y), max(v.z, v.w)); }
__device__ __forceinline__ float xmax(float x) {
  auto s = __builtin_amdgcn_permlane16_swap(bf(x), bf(x), false, false);
  x = fmaxf(fb(s[0]), fb(s[1]));
  auto t = __builtin_amdgcn_permlane32_swap(bf(x), bf(x), false, false);
  return fmaxf(fb(t[0]), fb(t[1]));
}
__device__ __forceinline__ float xsum(float x) {
  auto s = __builtin_amdgcn_permlane16_swap(bf(x), bf(x), false, false);
  x = fb(s[0]) + fb(s[1]);
  auto t = __builtin_amdgcn_permlane32_swap(bf(x), bf(x), false, false);
  return fb(t[0]) + fb(t[1]);
}
__device__ __forceinline__ void ld4_sc1(v4u (&r)[4], const u8* base, u32 voff) {
  asm volatile("global_load_dwordx4 %0, %4, %5 sc1\n\tglobal_load_dwordx4 %1, %4, %5 offset:1024 sc1\n\t"
               "global_load_dwordx4 %2, %4, %5 offset:2048 sc1\n\tglobal_load_dwordx4 %3, %4, %5 offset:3072 sc1\n\ts_waitcnt vmcnt(0)"
               : "=&v"(r[0]), "=&v"(r[1]), "=&v"(r[2]), "=&v"(r[3]) : "v"(voff), "s"(base) : "memory");
}
__device__ __forceinline__ void ld8_sc1(v4u (&r)[8], const u8* base, u32 voff, u32 voff2) {
  asm volatile("global_load_dwordx4 %0, %8, %10 sc1\n\tglobal_load_dwordx4 %1, %8, %10 offset:1024 sc1\n\t"
               "global_load_dwordx4 %2, %8, %10 offset:2048 sc1\n\tglobal_load_dwordx4 %3, %8, %10 offset:3072 sc1\n\t"
               "global_load_dwordx4 %4, %9, %10 sc1\n\tglobal_load_dwordx4 %5, %9, %10 offset:1024 sc1\n\t"
               "global_load_dwordx4 %6, %9, %10 offset:2048 sc1\n\tglobal_load_dwordx4 %7, %9, %10 offset:3072 sc1\n\ts_waitcnt vmcnt(0)"
               : "=&v"(r[0]), "=&v"(r[1]), "=&v"(r[2]), "=&v"(r[3]), "=&v"(r[4]), "=&v"(r[5]), "=&v"(r[6]), "=&v"(r[7])
               : "v"(voff), "v"(voff2), "s"(base) : "memory");
}
__device__ __forceinline__ void ld2_sc1(v4u& a, v4u& b, const u8* base, u32 voff) {
  asm volatile("global_load_dwordx4 %0, %2, %3 sc1\n\tglobal_load_dwordx4 %1, %2, %3 offset:16 sc1\n\ts_waitcnt vmcnt(0)"
               : "=&v"(a), "=&v"(b) : "v"(voff), "s"(base) : "memory");
}
__device__ __forceinline__ void ld1_sc1(v2u& a, const u8* base, u32 voff) {
  asm volatile("global_load_dwordx2 %0, %1, %2 sc1\n\ts_waitcnt vmcnt(0)" : "=&v"(a) : "v"(voff), "s"(base) : "memory");
}
#define CBAR() asm volatile("" ::: "memory")
#define SBAR() __builtin_amdgcn_sched_barrier(0)
template <int N> __device__ __forceinline__ void waitvm() { asm volatile("s_waitcnt vmcnt(%0)" ::"i"(N) : "memory"); }

__device__ __forceinline__ void flag_set(LAS u8* L, int f, int w, int lane) {
  asm volatile("s_waitcnt lgkmcnt(0)" ::: "memory");
  if (lane == 0) *(volatile LAS u32*)(L + L_F + f * 16 + w * 4) = 1u;
  CBAR();
}
__device__ __forceinline__ void flag_wait4(LAS u8* L, int f) {
#pragma nounroll
  for (int i = 0; i < SPIN_L; ++i) {
    v4u v = *(volatile LAS v4u*)(L + L_F + f * 16);
    if (__builtin_amdgcn_readfirstlane(v.x & v.y & v.z & v.w)) break;
    __builtin_amdgcn_s_sleep(1);
  }
  CBAR();
}
__device__ __forceinline__ void flag_wait1(LAS u8* L, int f, int w) {
#pragma nounroll
  for (int i = 0; i < SPIN_L; ++i) {
    u32 v = *(volatile LAS u32*)(L + L_F + f * 16 + w * 4);
    if (__builtin_amdgcn_readfirstlane(v)) break;
    __builtin_amdgcn_s_sleep(1);
  }
  CBAR();
}
#define STAMP(i) do { SBAR(); stamp[i] = __builtin_amdgcn_s_memrealtime(); SBAR(); } while (0)
#define STAMP_OUT() do { if (lane == 0) { for (int i_ = 0; i_ < 12; ++i_) ((GAS u64*)(ws + WS_STAMP))[(blockIdx.x * 8 + wave) * 16 + i_] = stamp[i_]; } } while (0)
#define MKRSRC(p, n) __builtin_amdgcn_make_buffer_rsrc((void*)(p), 0, (int)(n), 0x00020000)

__global__ void __launch_bounds__(512, 2)
gqa_fused(const u8* x, const u8* kc, const u8* vc, const u8* wq, const u8* wo, u8* out, u8* ws) {
  extern __shared__ __attribute__((aligned(16))) u8 smem[];
  LAS u8* L = (LAS u8*)smem;
  const int tid = threadIdx.x, lane = tid & 63, wave = __builtin_amdgcn_readfirstlane(tid >> 6);
  const int g = lane >> 4, c = lane & 15;
  const int bid = blockIdx.x, kvh = bid & 7, b = bid >> 5, split = (bid >> 3) & 3;
  const int wg = (b * 8 + kvh) * 4 + split;
  const __amdgpu_buffer_rsrc_t rs = MKRSRC(ws, WS_END);
  u64 stamp[12];
  for (int i = 0; i < 12; ++i) stamp[i] = 0;
  STAMP(0);

  if (wave < 4) {
    const int w = wave;
    __builtin_amdgcn_s_setprio(2);
    const size_t kvoff = ((size_t)((b * 8 + kvh) * 2048 + split * 512 + w * 128)) * 256;
    const u8* kb = kc + kvoff;
    const __amdgpu_buffer_rsrc_t rv = MKRSRC(vc + kvoff, 32768);
    u32 ko[4];
#pragma unroll
    for (int i = 0; i < 4; ++i) ko[i] = g * 256 + ((c ^ (4 * g + i)) * 16);
    const u32 ldsk = (u32)(size_t)L + L_K + w * 32768;
#pragma unroll
    for (int q4 = 0; q4 < 8; ++q4) {
#pragma unroll
      for (int i = 0; i < 4; ++i) {
        if (q4 * 4 + i >= THR) waitvm<THR - 1>();
        u32 keep;
        asm volatile("s_mov_b32 %0, m0\n\ts_mov_b32 m0, %3\n\ts_nop 0\n\tglobal_load_lds_dwordx4 %1, %2 offset:%4 nt\n\ts_mov_b32 m0, %0"
                     : "=&s"(keep) : "v"(ko[i]), "s"(kb + q4 * 4096), "s"(ldsk + q4 * 4096), "i"(i * 1024) : "memory");
      }
      if (q4 == 1) {
        if (w == 0) {
          *(volatile LAS u32*)(L + L_F + lane * 4) = 0u;
          asm volatile("s_waitcnt lgkmcnt(0)" ::: "memory");
        }
        SBAR();
        __builtin_amdgcn_s_barrier();
        SBAR();
      }
    }
    v4u v[32];
#pragma unroll
    for (int i = 0; i < 32; ++i) {
      waitvm<THR - 1>();
      if (i == THR - 1) { flag_set(L, F_K, w, lane); STAMP(1); }
      v[i] = __builtin_amdgcn_raw_buffer_load_b128(rv, lane * 16, i * 1024, 2);
    }
    SBAR();
    STAMP(2);
    flag_wait1(L, F_P, w);
    v4u p[4];
#pragma unroll
    for (int t = 0; t < 4; ++t) p[t] = *(LAS v4u*)(L + L_P + w * 4096 + t * 1024 + lane * 16);
    f4 o[8];
#pragma unroll
    for (int e = 0; e < 8; ++e) o[e] = f4{0.f, 0.f, 0.f, 0.f};
#pragma unroll
    for (int t = 0; t < 4; ++t) {
#pragma unroll
      for (int e = 0; e < 8; ++e) {
        v4u bfr;
#pragma unroll
        for (int jj = 0; jj < 4; ++jj) {
          const u32 a0 = v[8 * t + 2 * jj][e >> 1], a1 = v[8 * t + 2 * jj + 1][e >> 1];
          bfr[jj] = (e & 1) ? phi(a0, a1) : plo(a0, a1);
        }
        o[e] = mfma(p[t], bfr, o[e]);
      }
      SBAR();
    }
    STAMP(3);
#pragma unroll
    for (int r = 0; r < 4; ++r) {
      LAS u8* dst = L + L_K + w * 32768 + (4 * g + r) * 512 + c * 16;
      *(LAS f4*)dst = f4{o[0][r], o[1][r], o[2][r], o[3][r]};
      *(LAS f4*)(dst + 256) = f4{o[4][r], o[5][r], o[6][r], o[7][r]};
    }
    flag_set(L, F_O, w, lane);
    flag_wait4(L, F_O);
    {
      f4 s0 = {0.f, 0.f, 0.f, 0.f}, s1 = {0.f, 0.f, 0.f, 0.f};
#pragma unroll
      for (int ww = 0; ww < 4; ++ww) {
        LAS u8* src = L + L_K + ww * 32768 + (4 * w + g) * 512 + c * 16;
        s0 += *(LAS f4*)src;
        s1 += *(LAS f4*)(src + 256);
      }
      v4u ov = {san(pk(s0[0], s0[1])), san(pk(s0[2], s0[3])), san(pk(s1[0], s1[1])), san(pk(s1[2], s1[3]))};
      const u32 off = WS_BOX + ((b * 8 + kvh) * 4 + w) * BOX + 8192 + (((c >> 2) * 64 + (c & 3) * 16 + 4 * split + g) * 16);
      __builtin_amdgcn_raw_buffer_store_b128(ov, rs, off, 0, 16);
    }
    flag_set(L, F_SENT, w, lane);
    STAMP(4);
    if (w == 0) {
      __builtin_amdgcn_s_setprio(0);
      const u32 base = WS_BOX + wg * BOX + lane * 16;
      v4u r[8];
#pragma nounroll
      for (int it = 0; it < SPIN_G; ++it) {
        ld8_sc1(r, ws, base, base + 4096);
        u32 mx = 0;
#pragma unroll
        for (int k = 0; k < 8; ++k) mx = max(mx, umax4(r[k]));
        if (__all(mx != SENT)) break;
        __builtin_amdgcn_s_sleep(1);
      }
      f4 s = {0.f, 0.f, 0.f, 0.f};
#pragma unroll
      for (int k = 0; k < 8; ++k) s += __builtin_bit_cast(f4, r[k]);
#pragma unroll
      for (int j = 0; j < 4; ++j) s[j] = xsum(s[j]);
      const int i2 = kvh * 4 + split;
      if (lane < 16) {
        v2u ov = {pk(s[0], s[1]), pk(s[2], s[3])};
        *(GAS v2u*)(out + ((size_t)((b * 4 + (i2 >> 3)) * 512 + (i2 & 7) * 64 + 4 * c)) * 2) = ov;
      }
      STAMP(5);
    }
    STAMP_OUT();
    return;
  }

  const int cw = wave - 4, ct = tid - 256;
  const v2u ep = __builtin_amdgcn_raw_buffer_load_b64(rs, WS_EPOCH + (b >> 1) * 256, 0, 16);
  const int qi = (b & 1) * 4 + split;
  u32 tlo, thi;
  {
    v4u xa[4];
    v2u wv[32];
    const __amdgpu_buffer_rsrc_t rx = MKRSRC(x, 32768);
    const __amdgpu_buffer_rsrc_t rq = MKRSRC(wq, 4u << 20);
    const u32 xo = (((b & ~1) * 4 + (c & 7)) * 512 + cw * 128 + g * 8) * 2;
#pragma unroll
    for (int t = 0; t < 4; ++t) xa[t] = __builtin_amdgcn_raw_buffer_load_b128(rx, xo + t * 64, 0, 0);
    SBAR();
    __builtin_amdgcn_s_barrier();
    SBAR();
    const u32 wqo = ((cw * 128 + g * 8) * 4096 + kvh * 512 + qi * 64 + c * 4) * 2;
#pragma unroll
    for (int t = 0; t < 4; ++t)
#pragma unroll
      for (int j = 0; j < 8; ++j) wv[t * 8 + j] = __builtin_amdgcn_raw_buffer_load_b64(rq, wqo, (t * 32 + j) * 8192, W_AUX);
    SBAR();
    STAMP(11);
    {
      const v4u sv = {SENT, SENT, SENT, SENT};
      const u32 base = WS_BOX + wg * BOX + ct * 16;
#pragma unroll
      for (int k = 0; k < 3; ++k) __builtin_amdgcn_raw_buffer_store_b128(sv, rs, base + k * 4096, 0, 16);
      if (ct < 8) __builtin_amdgcn_raw_buffer_store_b128(sv, rs, base + 3 * 4096, 0, 16);
    }
    SBAR();
    f4 qa[4];
#pragma unroll
    for (int e = 0; e < 4; ++e) qa[e] = f4{0.f, 0.f, 0.f, 0.f};
#pragma unroll
    for (int t = 0; t < 4; ++t)
#pragma unroll
      for (int e = 0; e < 4; ++e) {
        v4u bfr;
#pragma unroll
        for (int jj = 0; jj < 4; ++jj) {
          const u32 a0 = wv[8 * t + 2 * jj][e >> 1], a1 = wv[8 * t + 2 * jj + 1][e >> 1];
          bfr[jj] = (e & 1) ? phi(a0, a1) : plo(a0, a1);
        }
        qa[e] = mfma(xa[t], bfr, qa[e]);
      }
    const u64 T = (((u64)(u32)__builtin_amdgcn_readfirstlane(ep.y) << 32) | (u32)__builtin_amdgcn_readfirstlane(ep.x)) + 1ull;
    tlo = (u32)(T << 16);
    thi = (u32)(T >> 16);
    if (lane < 32) {
#pragma unroll
      for (int r = 0; r < 4; ++r) *(LAS f4*)(L + L_QP + cw * 2048 + (g * 4 + r) * 256 + c * 16) = f4{qa[0][r], qa[1][r], qa[2][r], qa[3][r]};
    }
    SBAR();
    STAMP(8);
    waitvm<0>();
    STAMP(1);
    flag_set(L, F_QP, cw, lane);
    flag_wait4(L, F_QP);
    {
      const int row = ct >> 5, cp = ct & 31;
      f2 s = {0.f, 0.f};
#pragma unroll
      for (int ww = 0; ww < 4; ++ww) s += *(LAS f2*)(L + L_QP + ww * 2048 + row * 256 + cp * 8);
      const u32 pv = pk(s[0], s[1]);
      const v4u gr = {tlo | (pv & 0xFFFFu), thi, tlo | (pv >> 16), thi};
      const u32 off = WS_QBUF + ((((b & ~1) + (row >> 2)) * 8 + kvh) * 16 + (qi >> 1) * 4 + (row & 3)) * 1024 + ((qi & 1) * 64 + cp * 2) * 8;
      __builtin_amdgcn_raw_buffer_store_b128(gr, rs, off, 0, 16);
    }
    SBAR();
    STAMP(2);
  }
  v4u wv[32];
  const __amdgpu_buffer_rsrc_t ro = MKRSRC(wo + (size_t)(kvh * 4 + split) * 131072, 131072);
  const u32 woo = (g * 8 * 512 + cw * 128 + c * 8) * 2;
#define LDWO(i) __builtin_amdgcn_raw_buffer_load_b128(ro, woo, (((i) >> 3) * 32 + ((i) & 7)) * 1024, W_AUX)
#pragma unroll
  for (int i = 0; i < 16; ++i) wv[i] = LDWO(i);
  SBAR();
  {
    const u32 base = WS_QBUF + ((b * 8 + kvh) * 16 + cw * 4) * 1024 + lane * 16;
    v4u r[4];
#pragma nounroll
    for (int it = 0; it < SPIN_G; ++it) {
      ld4_sc1(r, ws, base);
      u32 bad = 0;
#pragma unroll
      for (int k = 0; k < 4; ++k) bad |= (((r[k].x ^ tlo) | (r[k].z ^ tlo)) & 0xFFFF0000u) | (r[k].y ^ thi) | (r[k].w ^ thi);
      if (__all(bad == 0)) break;
      __builtin_amdgcn_s_sleep(2);
    }
#pragma unroll
    for (int k = 0; k < 4; ++k)
      *(LAS u32*)(L + L_Q + ((lane >> 4) * 64 + ((lane >> 2) & 3) * 16 + cw * 4 + k) * 16 + (lane & 3) * 4) = plo(r[k].x, r[k].z);
    STAMP(3);
    flag_set(L, F_Q, cw, lane);
    flag_wait4(L, F_Q);
  }
  v4u qf[4];
#pragma unroll
  for (int s = 0; s < 4; ++s) qf[s] = *(LAS v4u*)(L + L_Q + s * 1024 + lane * 16);
  flag_wait1(L, F_K, cw);
  STAMP(4);
  f4 sc[8];
  {
    LAS u8* kr = L + L_K + cw * 32768 + (4 * (c & 3) + (c >> 2)) * 256;
    u32 co[4];
#pragma unroll
    for (int s = 0; s < 4; ++s) co[s] = ((4 * s + g) ^ c) * 16;
#pragma unroll
    for (int u = 0; u < 8; ++u) {
      sc[u] = f4{0.f, 0.f, 0.f, 0.f};
#pragma unroll
      for (int s = 0; s < 4; ++s) sc[u] = mfma(*(LAS v4u*)(kr + u * 4096 + co[s]), qf[s], sc[u]);
    }
  }
  float M;
  {
    float m = sc[0][0];
#pragma unroll
    for (int u = 0; u < 8; ++u)
#pragma unroll
      for (int r = 0; r < 4; ++r) m = fmaxf(m, sc[u][r]);
    m = xmax(m);
    if (lane < 16) *(LAS float*)(L + L_M + c * 16 + cw * 4) = m;
    flag_set(L, F_M, cw, lane);
    flag_wait4(L, F_M);
    const f4 mm = *(LAS f4*)(L + L_M + c * 16);
    M = fmaxf(fmaxf(mm[0], mm[1]), fmaxf(mm[2], mm[3])) * SCL;
    float l = 0.f;
#pragma unroll
    for (int u = 0; u < 8; ++u)
#pragma unroll
      for (int r = 0; r < 4; ++r) {
        sc[u][r] = __builtin_amdgcn_exp2f(__builtin_fmaf(sc[u][r], SCL, -M));
        l += sc[u][r];
      }
#pragma unroll
    for (int t = 0; t < 4; ++t) {
      const v4u pv = {pk(sc[2 * t][0], sc[2 * t][1]), pk(sc[2 * t][2], sc[2 * t][3]), pk(sc[2 * t + 1][0], sc[2 * t + 1][1]), pk(sc[2 * t + 1][2], sc[2 * t + 1][3])};
      *(LAS v4u*)(L + L_P + cw * 4096 + t * 1024 + lane * 16) = pv;
    }
    flag_set(L, F_P, cw, lane);
    STAMP(5);
    l = xsum(l);
    if (lane < 16) *(LAS float*)(L + L_L + c * 16 + cw * 4) = l;
    flag_set(L, F_L, cw, lane);
    if (cw == 0) {
      flag_wait4(L, F_L);
      if (lane < 16) {
        const f4 ll = *(LAS f4*)(L + L_L + c * 16);
        const v2u st = {san(bf(M)), san(bf((ll[0] + ll[1]) + (ll[2] + ll[3])))};
        const u32 off = WS_BOX + ((b * 8 + kvh) * 4 + (c >> 2)) * BOX + 12288 + (split * 4 + (c & 3)) * 8;
        __builtin_amdgcn_raw_buffer_store_b64(st, rs, off, 0, 16);
      }
    }
  }
  SBAR();
#pragma unroll
  for (int i = 16; i < 32; ++i) wv[i] = LDWO(i);
  SBAR();
  {
    const int qi2 = (lane >> 5) * 4 + (lane & 3);
    const u32 off = WS_QBUF + ((b * 8 + ((lane >> 2) & 7)) * 16 + (qi2 >> 1) * 4) * 1024 + (qi2 & 1) * 512;
#pragma nounroll
    for (int it = 0; it < SPIN_G; ++it) {
      v2u r;
      ld1_sc1(r, ws, off);
      if (__all((((r.x ^ tlo) & 0xFFFF0000u) | (r.y ^ thi)) == 0)) break;
      __builtin_amdgcn_s_sleep(4);
    }
    if (cw == 0 && (bid & 63) == 0 && lane == 0) {
      const v2u nt = {(tlo >> 16) | (thi << 16), thi >> 16};
      __builtin_amdgcn_raw_buffer_store_b64(nt, rs, WS_EPOCH + (b >> 1) * 256, 0, 16);
    }
  }
  STAMP(6);
  v4u wf[32];
#pragma unroll
  for (int t = 0; t < 4; ++t)
#pragma unroll
    for (int e = 0; e < 8; ++e)
#pragma unroll
      for (int jj = 0; jj < 4; ++jj) {
        const u32 a0 = wv[8 * t + 2 * jj][e >> 1], a1 = wv[8 * t + 2 * jj + 1][e >> 1];
        wf[t * 8 + e][jj] = (e & 1) ? phi(a0, a1) : plo(a0, a1);
      }
#pragma unroll
  for (int i = 0; i < 32; ++i) asm volatile("" : "+v"(wf[i]));
  float f[4];
  {
    const u32 off = WS_BOX + wg * BOX + 12288 + g * 32;
    v4u a, bq;
#pragma nounroll
    for (int it = 0; it < SPIN_G; ++it) {
      ld2_sc1(a, bq, ws, off);
      if (__all(max(umax4(a), umax4(bq)) != SENT)) break;
      __builtin_amdgcn_s_sleep(4);
    }
    const float ms[4] = {fb(a.x), fb(a.z), fb(bq.x), fb(bq.z)};
    const float ls[4] = {fb(a.y), fb(a.w), fb(bq.y), fb(bq.w)};
#pragma unroll
    for (int r = 0; r < 4; ++r) {
      const float mx = xmax(ms[r]);
      const float e = __builtin_amdgcn_exp2f(ms[r] - mx);
      f[r] = e / xsum(e * ls[r]);
    }
#pragma unroll
    for (int r = 0; r < 4; ++r) asm volatile("" : "+v"(f[r]));
  }
  STAMP(7);
  flag_wait4(L, F_SENT);
  {
    const u32 base = WS_BOX + wg * BOX + 8192 + lane * 16;
    v4u a[4];
#pragma nounroll
    for (int it = 0; it < SPIN_G; ++it) {
      ld4_sc1(a, ws, base);
      if (__all(max(max(umax4(a[0]), umax4(a[1])), max(umax4(a[2]), umax4(a[3]))) != SENT)) break;
    }
    STAMP(9);
    f4 d[8];
#pragma unroll
    for (int e = 0; e < 8; ++e) {
      d[e] = f4{0.f, 0.f, 0.f, 0.f};
#pragma unroll
      for (int t = 0; t < 4; ++t) d[e] = mfma(a[t], wf[t * 8 + e], d[e]);
    }
    u32 res[8];
#pragma unroll
    for (int e = 0; e < 8; ++e) {
      auto s02 = __builtin_amdgcn_permlane32_swap(bf(d[e][0] * f[0]), bf(d[e][2] * f[2]), false, false);
      auto s13 = __builtin_amdgcn_permlane32_swap(bf(d[e][1] * f[1]), bf(d[e][3] * f[3]), false, false);
      const float x02 = fb(s02[0]) + fb(s02[1]), x13 = fb(s13[0]) + fb(s13[1]);
      auto s = __builtin_amdgcn_permlane16_swap(bf(x02), bf(x13), false, false);
      res[e] = san(bf(fb(s[0]) + fb(s[1])));
    }
    const int i2 = g * 8 + cw * 2 + (c >> 3);
    const u32 off = WS_BOX + (b * 32 + i2) * BOX + (kvh * 4 + split) * 256 + (c & 7) * 32;
    __builtin_amdgcn_raw_buffer_store_b128(v4u{res[0], res[1], res[2], res[3]}, rs, off, 0, 16);
    __builtin_amdgcn_raw_buffer_store_b128(v4u{res[4], res[5], res[6], res[7]}, rs, off + 16, 0, 16);
    STAMP(10);
  }
  STAMP_OUT();
}

__global__ void probe(const u8* ws) {
  __shared__ u64 smn[256], smx[256], ssum[256]; __shared__ int scnt[256];
  const u64* st = (const u64*)(ws + WS_STAMP);
  const int t = threadIdx.x;
  u64 mn = ~0ull;
  for (int w = 0; w < 8; ++w) { u64 v = st[(t * 8 + w) * 16]; if (v < mn) mn = v; }
  smn[t] = mn; __syncthreads();
  if (t == 0) { for (int i = 1; i < 256; ++i) if (smn[i] < smn[0]) smn[0] = smn[i]; }
  __syncthreads(); mn = smn[0]; __syncthreads();
  u64 lo = ~0ull, hi = 0, sum = 0; int n = 0;
  if (0) { for (int w = 0; w < 8; ++w) for (int i = 0; i < 12; ++i) { u64 v = st[(t * 8 + w) * 16 + i]; if (v && v - mn > hi) hi = v - mn; } }
  else for (int w = 0 * 4; w < 0 * 4 + 4; ++w) { u64 v = st[(t * 8 + w) * 16 + 0]; if (!v) continue; u64 d = v - mn; sum += d; n++; if (d < lo) lo = d; if (d > hi) hi = d; }
  smn[t] = lo; smx[t] = hi; ssum[t] = sum; scnt[t] = n; __syncthreads();
  if (t == 0) {
    for (int i = 1; i < 256; ++i) { if (smn[i] < smn[0]) smn[0] = smn[i]; if (smx[i] > smx[0]) smx[0] = smx[i]; ssum[0] += ssum[i]; scnt[0] += scnt[i]; }
    u64 val = smx[0];
    if (val > 20000) val = 20000;
    const u64 t0 = __builtin_amdgcn_s_memrealtime();
    while (__builtin_amdgcn_s_memrealtime() - t0 < val * 100) __builtin_amdgcn_s_sleep(8);
  }
}
extern "C" void kernel_launch(void* const* d_in, const int* in_sizes, int n_in, void* d_out, int out_size, void* d_ws,
                              size_t ws_size, hipStream_t stream) {
  (void)in_sizes; (void)n_in; (void)out_size; (void)ws_size;
  (void)hipFuncSetAttribute((const void*)gqa_fused, hipFuncAttributeMaxDynamicSharedMemorySize, (int)LDS_BYTES);
  hipLaunchKernelGGL(gqa_fused, dim3(256), dim3(512), LDS_BYTES, stream, (const u8*)d_in[0], (const u8*)d_in[1], (const u8*)d_in[2],
                     (const u8*)d_in[3], (const u8*)d_in[4], (u8*)d_out, (u8*)d_ws);
  probe<<<1, 256, 0, stream>>>((const u8*)d_ws);
}
```

```cpp
#define QDELAY 32
#define WOLATE 0
#define HSTR 128
#define CSLEEP 1
#define HINTPOS 1
#define CFIRST 16
#define LFIRST 2
#define TA 8
#define TB 11
#include <hip/hip_runtime.h>
#include <stdint.h>

typedef unsigned u32;
typedef unsigned long long u64;
typedef unsigned char u8;
typedef u32 v4u __attribute__((ext_vector_type(4)));
typedef u32 v2u __attribute__((ext_vector_type(2)));
typedef float f4 __attribute__((ext_vector_type(4)));
typedef float f2 __attribute__((ext_vector_type(2)));
typedef short s8v __attribute__((ext_vector_type(8)));
typedef __bf16 bf2v __attribute__((ext_vector_type(2)));
#define GAS __attribute__((address_space(1)))
#define LAS __attribute__((address_space(3)))

#define THR1 8
#define NTH1 20
#define THR2 10
#define WOFLAG F_V
#define V2AT 24
#define CTHR 0
#define LPRIO2 0
#define LSLEEP 1
#define HDELAY 0
#define XFIRST 1
#define QIX 0
#define WROT 1
#define NOWQ 0
#define NOWO 0
#define NOQPUB 0
#define NOQPOLL 0
#define NOTAIL 0
#define CPAUSE -1
#define CPAT 4
#define QTRIG -1
#define FDELAY 8
#define L0WAIT 1
#define L0CNT 64
#define L0SLEEP 4
#define CSTART F_V
#define CDIRECT 16
#define STAMPS 0
#define SPING (1 << 17)

constexpr u32 WS_EPOCH = 0;
constexpr u32 WS_QBUF = 4096;
constexpr u32 WS_BOX = WS_QBUF + 64 * 8192;
constexpr u32 BOX = 16896;
constexpr u32 WS_END = WS_BOX + 256 * BOX;
constexpr u32 WS_STAMP = 8u << 20;
constexpr u32 L_K = 0;
constexpr u32 L_P = 131072;
constexpr u32 L_QP = L_P, L_Q = L_P + 16384;
constexpr u32 L_M = L_Q + 4096, L_L = L_M + 256, L_T = L_L + 256, L_F = L_T + 64;
constexpr u32 LDS_BYTES = L_F + 256;
constexpr u32 OSTR = 1088;
enum { F_QP = 0, F_Q, F_K, F_M, F_P, F_O, F_SENT, F_V, F_A, F_W, F_V2, F_K2 };

constexpr int SPIN_G = SPING;
constexpr int SPIN_L = 1 << 22;
constexpr float SCL = 0.08838834764831845f * 1.4426950408889634f;

__device__ __forceinline__ f4 mfma(v4u a, v4u b, f4 c) {
  return __builtin_amdgcn_mfma_f32_16x16x32_bf16(__builtin_bit_cast(s8v, a), __builtin_bit_cast(s8v, b), c, 0, 0, 0);
}
__device__ __forceinline__ u32 pk(float lo, float hi) {
  f2 v = {lo, hi};
  return __builtin_bit_cast(u32, __builtin_convertvector(v, bf2v));
}
__device__ __forceinline__ u32 plo(u32 a, u32 b) { return __builtin_amdgcn_perm(b, a, 0x05040100u); }
__device__ __forceinline__ u32 phi(u32 a, u32 b) { return __builtin_amdgcn_perm(b, a, 0x07060302u); }
__device__ __forceinline__ v4u tr8(const v4u* v, int e) {
  v4u r;
#pragma unroll
  for (int jj = 0; jj < 4; ++jj) {
    const u32 a0 = v[2 * jj][e >> 1], a1 = v[2 * jj + 1][e >> 1];
    r[jj] = (e & 1) ? phi(a0, a1) : plo(a0, a1);
  }
  return r;
}
__device__ __forceinline__ float fb(u32 x) { return __builtin_bit_cast(float, x); }
__device__ __forceinline__ u32 bf(float x) { return __builtin_bit_cast(u32, x); }
__device__ __forceinline__ float xmax(float x) {
  auto s = __builtin_amdgcn_permlane16_swap(bf(x), bf(x), false, false);
  x = fmaxf(fb(s[0]), fb(s[1]));
  auto t = __builtin_amdgcn_permlane32_swap(bf(x), bf(x), false, false);
  return fmaxf(fb(t[0]), fb(t[1]));
}
__device__ __forceinline__ float xsum(float x) {
  auto s = __builtin_amdgcn_permlane16_swap(bf(x), bf(x), false, false);
  x = fb(s[0]) + fb(s[1]);
  auto t = __builtin_amdgcn_permlane32_swap(bf(x), bf(x), false, false);
  return fb(t[0]) + fb(t[1]);
}
__device__ __forceinline__ float rsum4(f4 d) {
  auto s02 = __builtin_amdgcn_permlane32_swap(bf(d[0]), bf(d[2]), false, false);
  auto s13 = __builtin_amdgcn_permlane32_swap(bf(d[1]), bf(d[3]), false, false);
  const float x02 = fb(s02[0]) + fb(s02[1]), x13 = fb(s13[0]) + fb(s13[1]);
  auto s = __builtin_amdgcn_permlane16_swap(bf(x02), bf(x13), false, false);
  return fb(s[0]) + fb(s[1]);
}
template <int CTRL> __device__ __forceinline__ float dppf(float x) {
  return __builtin_bit_cast(float, __builtin_amdgcn_mov_dpp(__builtin_bit_cast(int, x), CTRL, 0xf, 0xf, true));
}
__device__ __forceinline__ u64 stale(v4u r, u32 tag) { return __builtin_amdgcn_ballot_w64(((r.y ^ tag) | (r.w ^ tag)) != 0u); }

__device__ __forceinline__ void poll2(v4u (&r)[2], u64 m0, u64 m1, const u8* base, u32 voff) {
  u64 sv;
  asm volatile("s_mov_b64 %2, exec\n\t"
               "s_mov_b64 exec, %5\n\tglobal_load_dwordx4 %0, %3, %4 sc1\n\t"
               "s_mov_b64 exec, %6\n\tglobal_load_dwordx4 %1, %3, %4 offset:1024 sc1\n\t"
               "s_mov_b64 exec, %2\n\ts_waitcnt vmcnt(0)"
               : "+v"(r[0]), "+v"(r[1]), "=&s"(sv) : "v"(voff), "s"(base), "s"(m0), "s"(m1) : "memory");
}
__device__ __forceinline__ void fetch_o(v4u (&r)[8], u32 want, const u8* base, u32 v0, u32 v1) {
  asm volatile("s_bitcmp1_b32 %11, 0\n\ts_cbranch_scc0 1f\n\t"
               "global_load_dwordx4 %0, %8, %10 sc1\n\tglobal_load_dwordx4 %1, %8, %10 offset:1024 sc1\n"
               "1:\n\ts_bitcmp1_b32 %11, 1\n\ts_cbranch_scc0 2f\n\t"
               "global_load_dwordx4 %2, %8, %10 offset:2048 sc1\n\tglobal_load_dwordx4 %3, %8, %10 offset:3072 sc1\n"
               "2:\n\ts_bitcmp1_b32 %11, 2\n\ts_cbranch_scc0 3f\n\t"
               "global_load_dwordx4 %4, %9, %10 sc1\n\tglobal_load_dwordx4 %5, %9, %10 offset:1024 sc1\n"
               "3:\n\ts_bitcmp1_b32 %11, 3\n\ts_cbranch_scc0 4f\n\t"
               "global_load_dwordx4 %6, %9, %10 offset:2048 sc1\n\tglobal_load_dwordx4 %7, %9, %10 offset:3072 sc1\n"
               "4:\n\ts_waitcnt vmcnt(0)"
               : "+v"(r[0]), "+v"(r[1]), "+v"(r[2]), "+v"(r[3]), "+v"(r[4]), "+v"(r[5]), "+v"(r[6]), "+v"(r[7])
               : "v"(v0), "v"(v1), "s"(base), "s"(want) : "memory", "scc");
}
__device__ __forceinline__ void fetch8(v4u (&r)[8], u32 want, const u8* base, u32 v0, u32 v1) {
  asm volatile("s_bitcmp1_b32 %11, 0\n\ts_cbranch_scc0 1f\n\tglobal_load_dwordx4 %0, %8, %10 sc1\n"
               "1:\n\ts_bitcmp1_b32 %11, 1\n\ts_cbranch_scc0 2f\n\tglobal_load_dwordx4 %1, %8, %10 offset:1024 sc1\n"
               "2:\n\ts_bitcmp1_b32 %11, 2\n\ts_cbranch_scc0 3f\n\tglobal_load_dwordx4 %2, %8, %10 offset:2048 sc1\n"
               "3:\n\ts_bitcmp1_b32 %11, 3\n\ts_cbranch_scc0 4f\n\tglobal_load_dwordx4 %3, %8, %10 offset:3072 sc1\n"
               "4:\n\ts_bitcmp1_b32 %11, 4\n\ts_cbranch_scc0 5f\n\tglobal_load_dwordx4 %4, %9, %10 sc1\n"
               "5:\n\ts_bitcmp1_b32 %11, 5\n\ts_cbranch_scc0 6f\n\tglobal_load_dwordx4 %5, %9, %10 offset:1024 sc1\n"
               "6:\n\ts_bitcmp1_b32 %11, 6\n\ts_cbranch_scc0 7f\n\tglobal_load_dwordx4 %6, %9, %10 offset:2048 sc1\n"
               "7:\n\ts_bitcmp1_b32 %11, 7\n\ts_cbranch_scc0 8f\n\tglobal_load_dwordx4 %7, %9, %10 offset:3072 sc1\n"
               "8:\n\ts_waitcnt vmcnt(0)"
               : "+v"(r[0]), "+v"(r[1]), "+v"(r[2]), "+v"(r[3]), "+v"(r[4]), "+v"(r[5]), "+v"(r[6]), "+v"(r[7])
               : "v"(v0), "v"(v1), "s"(base), "s"(want) : "memory", "scc");
}
__device__ __forceinline__ void ld8_issue(v4u (&r)[8], const u8* base, u32 v0, u32 v1) {
  asm volatile("global_load_dwordx4 %0, %8, %10 sc1\n\tglobal_load_dwordx4 %1, %8, %10 offset:1024 sc1\n\t"
               "global_load_dwordx4 %2, %8, %10 offset:2048 sc1\n\tglobal_load_dwordx4 %3, %8, %10 offset:3072 sc1\n\t"
               "global_load_dwordx4 %4, %9, %10 sc1\n\tglobal_load_dwordx4 %5, %9, %10 offset:1024 sc1\n\t"
               "global_load_dwordx4 %6, %9, %10 offset:2048 sc1\n\tglobal_load_dwordx4 %7, %9, %10 offset:3072 sc1"
               : "=&v"(r[0]), "=&v"(r[1]), "=&v"(r[2]), "=&v"(r[3]), "=&v"(r[4]), "=&v"(r[5]), "=&v"(r[6]), "=&v"(r[7])
               : "v"(v0), "v"(v1), "s"(base) : "memory");
}
__device__ __forceinline__ void ld8_wait(v4u (&r)[8]) {
  asm volatile("s_waitcnt vmcnt(0)"
               : "+v"(r[0]), "+v"(r[1]), "+v"(r[2]), "+v"(r[3]), "+v"(r[4]), "+v"(r[5]), "+v"(r[6]), "+v"(r[7]) :: "memory");
}
__device__ __forceinline__ void ld1(v4u& r, const u8* base, u32 v0) {
  asm volatile("global_load_dwordx4 %0, %1, %2 sc1\n\ts_waitcnt vmcnt(0)" : "=&v"(r) : "v"(v0), "s"(base) : "memory");
}
#define CBAR() asm volatile("" ::: "memory")
#define SBAR() __builtin_amdgcn_sched_barrier(0)
constexpr int thr_of(int n) { return TA + (TB - TA) * n / 63; }
template <int N> __device__ __forceinline__ void waitvm() { asm volatile("s_waitcnt vmcnt(%0)" ::"i"(N) : "memory"); }

__device__ __forceinline__ void flag_set(LAS u8* L, int f, int w, int lane) {
  CBAR();
  if (lane == 0) *(volatile LAS u32*)(L + L_F + f * 16 + w * 4) = 1u;
  CBAR();
}
template <int SLEEP> __device__ __forceinline__ void flag_wait4(LAS u8* L, int f) {
#pragma nounroll
  for (int i = 0; i < SPIN_L; ++i) {
    v4u v = *(volatile LAS v4u*)(L + L_F + f * 16);
    if (__builtin_amdgcn_readfirstlane(v.x & v.y & v.z & v.w)) break;
    if (SLEEP) __builtin_amdgcn_s_sleep(SLEEP);
  }
  CBAR();
}
template <int SLEEP> __device__ __forceinline__ void flag_wait1(LAS u8* L, int f, int w) {
#pragma nounroll
  for (int i = 0; i < SPIN_L; ++i) {
    u32 v = *(volatile LAS u32*)(L + L_F + f * 16 + w * 4);
    if (__builtin_amdgcn_readfirstlane(v)) break;
    if (SLEEP) __builtin_amdgcn_s_sleep(SLEEP);
  }
  CBAR();
}
#define STAMP(i) do { } while (0)
#define STAMP_OUT() do { } while (0)
#define MKRSRC(p, n) __builtin_amdgcn_make_buffer_rsrc((void*)(p), 0, (int)(n), 0x00020000)

__global__ void __launch_bounds__(512, 2)
gqa_fused(const u8* x, const u8* kc, const u8* vc, const u8* wq, const u8* wo, u8* out, u8* ws) {
  extern __shared__ __attribute__((aligned(16))) u8 smem[];
  LAS u8* L = (LAS u8*)smem;
  const int tid = threadIdx.x, lane = tid & 63, wave = __builtin_amdgcn_readfirstlane(tid >> 6);
  const int g = lane >> 4, c = lane & 15;
  const int bid = blockIdx.x, kvh = bid & 7, j = bid >> 3, b = j >> 2, split = j & 3;
  const int wg = kvh * 32 + j;
  const int bh = j >> 4, hl = (j >> 2) & 3, nq = j & 3;
  const __amdgpu_buffer_rsrc_t rs = MKRSRC(ws, WS_END);
  STAMP(0);

  if (wave < 4) {
    const int w = wave;
    const int r8 = (w + 4 * (j & 1) + (j >> 1)) & 7;
    const size_t kvoff = ((size_t)((b * 8 + kvh) * 2048 + split * 512 + w * 128)) * 256;
    const u8* kb = kc + kvoff;
    const __amdgpu_buffer_rsrc_t rv = MKRSRC(vc + kvoff, 32768);
    u32 ko[4];
#pragma unroll
    for (int i = 0; i < 4; ++i) ko[i] = g * 256 + ((c ^ (4 * g + i)) * 16);
    const u32 ldsk = (u32)(size_t)L + L_K + w * 32768;
#pragma unroll
    for (int q4 = 0; q4 < 8; ++q4) {
#pragma unroll
      for (int i = 0; i < 4; ++i) {
        const int n = q4 * 4 + i;
        if (n >= thr_of(n)) asm volatile("s_waitcnt vmcnt(%0)" ::"i"(thr_of(q4 * 4 + i) - 1) : "memory");
        u32 keep;
        asm volatile("s_mov_b32 %0, m0\n\ts_mov_b32 m0, %3\n\ts_nop 0\n\tglobal_load_lds_dwordx4 %1, %2 offset:%4 nt\n\ts_mov_b32 m0, %0"
                     : "=&s"(keep) : "v"(ko[i]), "s"(kb + ((q4 + r8) & 7) * 4096), "s"(ldsk + q4 * 4096), "i"(i * 1024) : "memory");
        if (n == QTRIG) flag_set(L, F_K2, w, lane);
        if (n == LFIRST - 1) {
          if (w == 0) {
            *(volatile LAS u32*)(L + L_F + lane * 4) = 0u;
            asm volatile("s_waitcnt lgkmcnt(0)" ::: "memory");
          }
          SBAR();
          __builtin_amdgcn_s_barrier();
          SBAR();
        }
      }
    }
    v4u v[32];
#pragma unroll
    for (int i = 0; i < 32; ++i) {
      asm volatile("s_waitcnt vmcnt(%0)" ::"i"(thr_of(32 + i) - 1) : "memory");
      if (i >= thr_of(32 + i) - 1 && (i == 0 || i - 1 < thr_of(31 + i) - 1)) { flag_set(L, F_K, w, lane); STAMP(1); }
      if (i == V2AT) flag_set(L, F_V2, w, lane);
      v[i] = __builtin_amdgcn_raw_buffer_load_b128(rv, lane * 16 + (i & 3) * 1024, (((i >> 2) + r8) & 7) * 4096, 2);
    }
    SBAR();
    flag_set(L, F_V, w, lane);
    STAMP(2);
    flag_wait4<1>(L, F_P);
    __builtin_amdgcn_s_setprio(3);
    v4u p[4];
#pragma unroll
    for (int t = 0; t < 4; ++t) p[t] = *(LAS v4u*)(L + L_P + w * 4096 + t * 1024 + lane * 16);
    const u32 tag = *(LAS u32*)(L + L_T);
    v4u stv;
    {
      const f4 mm = *(LAS f4*)(L + L_M + (4 * w + (lane & 3)) * 16), ll = *(LAS f4*)(L + L_L + (4 * w + (lane & 3)) * 16);
      stv = v4u{bf(fmaxf(fmaxf(mm[0], mm[1]), fmaxf(mm[2], mm[3])) * SCL), tag, bf((ll[0] + ll[1]) + (ll[2] + ll[3])), tag};
    }
    STAMP(6);
    f4 o[8];
#pragma unroll
    for (int e = 0; e < 8; ++e) o[e] = f4{0.f, 0.f, 0.f, 0.f};
#pragma unroll
    for (int t = 0; t < 4; ++t) {
#pragma unroll
      for (int e = 0; e < 8; ++e) o[e] = mfma(tr8(&v[8 * t], e), p[t], o[e]);
      SBAR();
    }
    STAMP(3);
#pragma unroll
    for (int e = 0; e < 8; ++e) *(LAS f4*)(L + L_K + w * 32768 + e * OSTR + lane * 16) = o[e];
    if (NOTAIL) { STAMP_OUT(); return; }
    flag_set(L, F_O, w, lane);
    const u32 obase = WS_BOX + (kvh * 32 + b * 4 + w) * BOX + 8192;
    flag_wait4<0>(L, F_O);
    {
      LAS u8* src = L + L_K + ((lane >> 2) & 7) * OSTR + ((lane >> 5) * 16 + 4 * w + (lane & 3)) * 16;
      f4 s0 = *(LAS f4*)src, s1 = *(LAS f4*)(src + 512);
#pragma unroll
      for (int ww = 1; ww < 4; ++ww) {
        s0 += *(LAS f4*)(src + ww * 32768);
        s1 += *(LAS f4*)(src + ww * 32768 + 512);
      }
      const u32 off = obase + (lane >> 5) * 2048 + ((lane >> 2) & 1) * 1024 + (((lane >> 3) & 3) * 16 + 4 * split + (lane & 3)) * 16;
      __builtin_amdgcn_raw_buffer_store_b128(v4u{pk(s0[0], s0[1]), tag, pk(s0[2], s0[3]), tag}, rs, off, 0, 16);
      __builtin_amdgcn_raw_buffer_store_b128(v4u{pk(s1[0], s1[1]), tag, pk(s1[2], s1[3]), tag}, rs, off + 4096, 0, 16);
      SBAR();
      if (lane < 4) __builtin_amdgcn_raw_buffer_store_b128(stv, rs, obase + 8192 + split * HSTR + lane * 16, 0, 16);
    }
    STAMP(4);
    if (w == 0) {
      __builtin_amdgcn_s_setprio(0);
      const u32 base = WS_BOX + wg * BOX + lane * 16;
      v4u r[8];
#pragma unroll
      for (int k = 0; k < 8; ++k) r[k] = v4u{0u, 0u, 0u, 0u};
      u32 todo = 255u;
      flag_wait1<2>(L, F_A, (kvh * 4 + hl) >> 3);
      __builtin_amdgcn_s_sleep(FDELAY);
#pragma nounroll
      for (int it = 0; it < SPIN_G; ++it) {
        fetch8(r, todo, ws, base, base + 4096);
        u32 nt = 0;
#pragma unroll
        for (int k = 0; k < 8; ++k) nt |= stale(r[k], tag) ? (1u << k) : 0u;
        todo = nt;
        if (!todo) break;
      }
      f4 s = {0.f, 0.f, 0.f, 0.f};
#pragma unroll
      for (int k = 0; k < 8; ++k) s += f4{fb(r[k].x << 16), fb(r[k].x & 0xFFFF0000u), fb(r[k].z << 16), fb(r[k].z & 0xFFFF0000u)};
      const float tot = rsum4(s);
      const int u = kvh * 4 + hl, row16 = u >> 1, eh = u & 1;
      const __bf16 ob = (__bf16)tot;
      *(GAS unsigned short*)(out + ((size_t)((bh * 16 + row16) * 512 + nq * 128 + 8 * c + 4 * eh + g)) * 2) = __builtin_bit_cast(unsigned short, ob);
      if ((bid & 127) == 0 && lane == 0) __builtin_amdgcn_raw_buffer_store_b32(tag, rs, WS_EPOCH + bh * 256, 0, 16);
      STAMP(5);
    }
    STAMP_OUT();
    return;
  }

  const int cw = wave - 4, ct = tid - 256;
  __builtin_amdgcn_s_setprio(3);
  const int qi = (j ^ QIX) & 7;
  const int qrot = WROT ? (j >> 3) * 4 : 0;
  u32 tag;
  v4u wv[16];
  {
    v4u xa[2];
    const u32 ep = __builtin_amdgcn_raw_buffer_load_b32(rs, ct == 0 ? WS_EPOCH + bh * 256 : 0x7FFFFFF0u, 0, 16);
    const __amdgpu_buffer_rsrc_t rx = MKRSRC(x, 32768);
    const __amdgpu_buffer_rsrc_t rq = MKRSRC(wq, NOWQ ? 0 : (4u << 20));
    const u32 xo = (((b & ~1) * 4 + (c & 7)) * 512 + cw * 128 + (2 * g + (c >> 3)) * 8) * 2;
#pragma unroll
    for (int T = 0; T < 2; ++T) xa[T] = __builtin_amdgcn_raw_buffer_load_b128(rx, xo, (T ^ (qrot >> 3)) * 128, 0);
    SBAR();
    const u32 wqo = ((cw * 128 + (lane >> 3) * 8) * 4096 + kvh * 512 + qi * 64 + (lane & 7) * 8) * 2;
#pragma unroll
    for (int i = 0; i < 16; ++i) {
      if (CTHR && i >= CTHR) waitvm<CTHR ? CTHR - 1 : 0>();
      const int ii = i ^ qrot;
      wv[i] = __builtin_amdgcn_raw_buffer_load_b128(rq, wqo, ((ii >> 3) * 64 + (ii & 7)) * 8192, 2);
      if (i == CFIRST - 1) { SBAR(); __builtin_amdgcn_s_barrier(); SBAR(); }
      if (CPAUSE >= 0 && i == CPAT - 1) waitvm<CPAUSE >= 0 ? CPAUSE : 0>();
    }
    SBAR();
    STAMP(11);
    if (qrot & 4) {
#pragma unroll
      for (int T = 0; T < 2; ++T) xa[T] = v4u{xa[T].z, xa[T].w, xa[T].x, xa[T].y};
    }
    f4 qa[8];
#pragma unroll
    for (int e = 0; e < 8; ++e) qa[e] = f4{0.f, 0.f, 0.f, 0.f};
#pragma unroll
    for (int T = 0; T < 2; ++T)
#pragma unroll
      for (int e = 0; e < 8; ++e) qa[e] = mfma(xa[T], tr8(&wv[8 * T], e), qa[e]);
    if ((g >> 1) == (c >> 3)) {
#pragma unroll
      for (int r = 0; r < 4; ++r) {
        LAS u8* dst = L + L_QP + cw * 4096 + (g * 4 + r) * 256 + (c & 7) * 32;
        *(LAS f4*)dst = f4{qa[0][r], qa[1][r], qa[2][r], qa[3][r]};
        *(LAS f4*)(dst + 16) = f4{qa[4][r], qa[5][r], qa[6][r], qa[7][r]};
      }
    }
    if (ct == 0) *(LAS u32*)(L + L_T) = ep + 1u;
    flag_set(L, F_QP, cw, lane);
    flag_wait4<0>(L, F_QP);
    tag = __builtin_amdgcn_readfirstlane(*(LAS u32*)(L + L_T));
    if (cw < 2 && !NOQPUB) {
      const int row = ct >> 4;
      f4 s = {0.f, 0.f, 0.f, 0.f};
#pragma unroll
      for (int ww = 0; ww < 8; ++ww) s += *(LAS f4*)(L + L_QP + ww * 2048 + row * 256 + c * 16);
      const u32 off = WS_QBUF + ((((b & ~1) + (row >> 2)) * 8 + kvh) * 16 + (qi >> 1) * 4 + (row & 3)) * 512 + (qi & 1) * 256 + c * 16;
      __builtin_amdgcn_raw_buffer_store_b128(v4u{pk(s[0], s[1]), tag, pk(s[2], s[3]), tag}, rs, off, 0, 16);
    }
    SBAR();
    STAMP(2);
  }
  {
    const __amdgpu_buffer_rsrc_t ro = MKRSRC(wo + (size_t)(kvh * 4 + hl) * 131072, NOWO ? 0 : 131072);
    const u32 woo = (cw * 32 + 2 * g) * 1024 + nq * 256 + c * 16;
#pragma unroll
    for (int i = 0; i < 8; ++i) wv[i] = __builtin_amdgcn_raw_buffer_load_b128(ro, woo, ((i & 3) * 8 + ((i >> 2) ^ (WROT ? bh : 0))) * 1024, 2);
    SBAR();
  }
  __builtin_amdgcn_s_sleep(QDELAY);
  {
    const u32 base = WS_QBUF + ((b * 8 + kvh) * 16 + cw * 4) * 512 + lane * 16;
    v4u r[2] = {v4u{0u, 0u, 0u, 0u}, v4u{0u, 0u, 0u, 0u}};
    u64 m0 = ~0ull, m1 = ~0ull;
#pragma nounroll
    for (int it = 0; it < (NOQPOLL ? 0 : NOQPUB ? 1 : SPIN_G); ++it) {
      poll2(r, m0, m1, ws, base);
      m0 = stale(r[0], tag);
      m1 = stale(r[1], tag);
      if (!(m0 | m1)) break;
    }
    LAS u8* dst = L + L_Q + (((lane >> 3) & 3) * 64 + ((lane >> 1) & 3) * 16 + cw * 4 + (lane >> 5)) * 16 + (lane & 1) * 8;
    *(LAS v2u*)dst = v2u{r[0].x, r[0].z};
    *(LAS v2u*)(dst + 32) = v2u{r[1].x, r[1].z};
    STAMP(3);
    flag_set(L, F_Q, cw, lane);
    flag_wait4<1>(L, F_Q);
  }
  v4u qf[4];
#pragma unroll
  for (int s = 0; s < 4; ++s) qf[s] = *(LAS v4u*)(L + L_Q + s * 1024 + lane * 16);
  flag_wait1<1>(L, F_K, cw);
  STAMP(4);
  f4 sc[8];
  {
    LAS u8* kr = L + L_K + cw * 32768 + (4 * (c & 3) + (c >> 2)) * 256;
    v4u kf[32];
#pragma unroll
    for (int u = 0; u < 8; ++u)
#pragma unroll
      for (int s = 0; s < 4; ++s) kf[u * 4 + s] = *(LAS v4u*)(kr + u * 4096 + ((4 * s + g) ^ c) * 16);
    SBAR();
#pragma unroll
    for (int u = 0; u < 8; ++u) {
      sc[u] = f4{0.f, 0.f, 0.f, 0.f};
#pragma unroll
      for (int s = 0; s < 4; ++s) sc[u] = mfma(kf[u * 4 + s], qf[s], sc[u]);
    }
  }
  {
    float m = sc[0][0];
#pragma unroll
    for (int u = 0; u < 8; ++u)
#pragma unroll
      for (int r = 0; r < 4; ++r) m = fmaxf(m, sc[u][r]);
    m = xmax(m);
    if (lane < 16) *(LAS float*)(L + L_M + c * 16 + cw * 4) = m;
    flag_set(L, F_M, cw, lane);
#pragma unroll
    for (int e = 0; e < 8; ++e) {
      const v4u fr = tr8(wv, e);
      LAS u8* d = L + L_K + cw * 32768 + 16384 + e * 1024 + lane * 16;
      *(LAS v2u*)(d + (WROT ? bh * 8 : 0)) = v2u{fr.x, fr.y};
      *(LAS v2u*)(d + (WROT ? 8 - bh * 8 : 8)) = v2u{fr.z, fr.w};
    }
    flag_wait4<0>(L, F_M);
    const f4 mm = *(LAS f4*)(L + L_M + c * 16);
    const float M = fmaxf(fmaxf(mm[0], mm[1]), fmaxf(mm[2], mm[3])) * SCL;
    float l = 0.f;
#pragma unroll
    for (int u = 0; u < 8; ++u)
#pragma unroll
      for (int r = 0; r < 4; ++r) {
        sc[u][r] = __builtin_amdgcn_exp2f(__builtin_fmaf(sc[u][r], SCL, -M));
        l += sc[u][r];
      }
    l = xsum(l);
    if (lane < 16) *(LAS float*)(L + L_L + c * 16 + cw * 4) = l;
#pragma unroll
    for (int t = 0; t < 4; ++t) {
      const v4u pv = {pk(sc[2 * t][0], sc[2 * t][1]), pk(sc[2 * t][2], sc[2 * t][3]), pk(sc[2 * t + 1][0], sc[2 * t + 1][1]), pk(sc[2 * t + 1][2], sc[2 * t + 1][3])};
      *(LAS v4u*)(L + L_P + cw * 4096 + t * 1024 + lane * 16) = pv;
    }
    flag_set(L, F_P, cw, lane);
    STAMP(5);
  }
  flag_set(L, F_W, cw, lane);
  __builtin_amdgcn_s_setprio(0);
  flag_wait4<1>(L, F_W);
  v4u wf[32];
#pragma unroll
  for (int t = 0; t < 4; ++t)
#pragma unroll
    for (int e = 0; e < 8; ++e) wf[t * 8 + e] = *(LAS v4u*)(L + L_K + t * 32768 + 16384 + e * 1024 + lane * 16);
#pragma unroll
  for (int i = 0; i < 32; ++i) asm volatile("" : "+v"(wf[i]));
  STAMP(6);
  if (NOTAIL) { STAMP_OUT(); return; }
  flag_wait4<2>(L, CSTART);
  STAMP(8);
  {
    const u32 obase = WS_BOX + (kvh * 32 + (bh * 4 + cw) * 4 + hl) * BOX + 8192;
    const u32 v0 = obase + lane * 16;
    const u32 vs = obase + 8192 + g * HSTR + (c & 3) * 16;
    const int u = (cw * 4 + g) * 2;
    const u32 off = WS_BOX + ((u >> 2) * 32 + bh * 16 + (u & 3) * 4 + nq) * BOX + (kvh * 4 + hl) * 256 + c * 16;
    v4u st;
#pragma nounroll
    for (int it = 0; it < SPIN_G; ++it) {
      ld1(st, ws, vs);
      if (!stale(st, tag)) break;
      __builtin_amdgcn_s_sleep(CSLEEP);
    }
    __builtin_amdgcn_s_setprio(3);
    if (HDELAY) __builtin_amdgcn_s_sleep(HDELAY);
    STAMP(7);
    float res[8];
#pragma nounroll
    for (int it = 0; it < SPIN_G; ++it) {
      v4u r[8];
      ld8_issue(r, ws, v0, v0 + 4096);
      SBAR();
      const float mx = xmax(fb(st.x));
      const float ex = __builtin_amdgcn_exp2f(fb(st.x) - mx);
      const float fq = ex * __builtin_amdgcn_rcpf(xsum(ex * fb(st.z)));
      f4 f = {dppf<0x00>(fq), dppf<0x55>(fq), dppf<0xAA>(fq), dppf<0xFF>(fq)};
      asm volatile("" : "+v"(f));
      ld8_wait(r);
      STAMP(9);
      v4u a[4];
#pragma unroll
      for (int t = 0; t < 4; ++t) a[t] = v4u{r[2 * t].x, r[2 * t].z, r[2 * t + 1].x, r[2 * t + 1].z};
      u32 bad = 0;
#pragma unroll
      for (int e = 0; e < 8; ++e) {
        f4 d = {0.f, 0.f, 0.f, 0.f};
#pragma unroll
        for (int t = 0; t < 4; ++t) d = mfma(a[t], wf[t * 8 + e], d);
        bad |= (r[e].y ^ tag) | (r[e].w ^ tag);
        res[e] = rsum4(d * f);
      }
      if (!__builtin_amdgcn_ballot_w64(bad != 0u)) break;
    }
    __builtin_amdgcn_raw_buffer_store_b128(v4u{pk(res[0], res[1]), tag, pk(res[2], res[3]), tag}, rs, off, 0, 16);
    __builtin_amdgcn_raw_buffer_store_b128(v4u{pk(res[4], res[5]), tag, pk(res[6], res[7]), tag}, rs, off + 4 * BOX, 0, 16);
    flag_set(L, F_A, cw, lane);
    STAMP(10);
  }
  STAMP_OUT();
}

extern "C" void kernel_launch(void* const* d_in, const int* in_sizes, int n_in, void* d_out, int out_size, void* d_ws,
                              size_t ws_size, hipStream_t stream) {
  (void)in_sizes; (void)n_in; (void)out_size; (void)ws_size;
  (void)hipFuncSetAttribute((const void*)gqa_fused, hipFuncAttributeMaxDynamicSharedMemorySize, (int)LDS_BYTES);
  hipLaunchKernelGGL(gqa_fused, dim3(256), dim3(512), LDS_BYTES, stream, (const u8*)d_in[0], (const u8*)d_in[1], (const u8*)d_in[2],
                     (const u8*)d_in[3], (const u8*)d_in[4], (u8*)d_out, (u8*)d_ws);
}
```

```cpp
#define QDELAY 32
#define WOLATE 0
#define HSTR 128
#define CSLEEP 1
#define HINTPOS 1
#define CFIRST 16
#define LFIRST 2
#define TA 8
#define TB 11
#include <hip/hip_runtime.h>
#include <stdint.h>

typedef unsigned u32;
typedef unsigned long long u64;
typedef unsigned char u8;
typedef u32 v4u __attribute__((ext_vector_type(4)));
typedef u32 v2u __attribute__((ext_vector_type(2)));
typedef float f4 __attribute__((ext_vector_type(4)));
typedef float f2 __attribute__((ext_vector_type(2)));
typedef short s8v __attribute__((ext_vector_type(8)));
typedef __bf16 bf2v __attribute__((ext_vector_type(2)));
#define GAS __attribute__((address_space(1)))
#define LAS __attribute__((address_space(3)))

#define THR1 8
#define NTH1 20
#define THR2 10
#define WOFLAG F_V
#define V2AT 24
#define CTHR 0
#define LPRIO2 0
#define LSLEEP 1
#define HDELAY 0
#define XFIRST 1
#define QIX 0
#define WROT 1
#define NOWQ 0
#define NOWO 0
#define NOQPUB 0
#define NOQPOLL 0
#define NOTAIL 0
#define CPAUSE -1
#define CPAT 4
#define QTRIG -1
#define FDELAY 8
#define L0WAIT 1
#define L0CNT 64
#define L0SLEEP 4
#define CSTART F_V
#define CDIRECT 16
#define STAMPS 1
#define SPING (1 << 17)

constexpr u32 WS_EPOCH = 0;
constexpr u32 WS_QBUF = 4096;
constexpr u32 WS_BOX = WS_QBUF + 64 * 8192;
constexpr u32 BOX = 16896;
constexpr u32 WS_END = WS_BOX + 256 * BOX;
constexpr u32 WS_STAMP = 8u << 20;
constexpr u32 L_K = 0;
constexpr u32 L_P = 131072;
constexpr u32 L_QP = L_P, L_Q = L_P + 16384;
constexpr u32 L_M = L_Q + 4096, L_L = L_M + 256, L_T = L_L + 256, L_F = L_T + 64;
constexpr u32 LDS_BYTES = L_F + 256;
constexpr u32 OSTR = 1088;
enum { F_QP = 0, F_Q, F_K, F_M, F_P, F_O, F_SENT, F_V, F_A, F_W, F_V2, F_K2 };

constexpr int SPIN_G = SPING;
constexpr int SPIN_L = 1 << 22;
constexpr float SCL = 0.08838834764831845f * 1.4426950408889634f;

__device__ __forceinline__ f4 mfma(v4u a, v4u b, f4 c) {
  return __builtin_amdgcn_mfma_f32_16x16x32_bf16(__builtin_bit_cast(s8v, a), __builtin_bit_cast(s8v, b), c, 0, 0, 0);
}
__device__ __forceinline__ u32 pk(float lo, float hi) {
  f2 v = {lo, hi};
  return __builtin_bit_cast(u32, __builtin_convertvector(v, bf2v));
}
__device__ __forceinline__ u32 plo(u32 a, u32 b) { return __builtin_amdgcn_perm(b, a, 0x05040100u); }
__device__ __forceinline__ u32 phi(u32 a, u32 b) { return __builtin_amdgcn_perm(b, a, 0x07060302u); }
__device__ __forceinline__ v4u tr8(const v4u* v, int e) {
  v4u r;
#pragma unroll
  for (int jj = 0; jj < 4; ++jj) {
    const u32 a0 = v[2 * jj][e >> 1], a1 = v[2 * jj + 1][e >> 1];
    r[jj] = (e & 1) ? phi(a0, a1) : plo(a0, a1);
  }
  return r;
}
__device__ __forceinline__ float fb(u32 x) { return __builtin_bit_cast(float, x); }
__device__ __forceinline__ u32 bf(float x) { return __builtin_bit_cast(u32, x); }
__device__ __forceinline__ float xmax(float x) {
  auto s = __builtin_amdgcn_permlane16_swap(bf(x), bf(x), false, false);
  x = fmaxf(fb(s[0]), fb(s[1]));
  auto t = __builtin_amdgcn_permlane32_swap(bf(x), bf(x), false, false);
  return fmaxf(fb(t[0]), fb(t[1]));
}
__device__ __forceinline__ float xsum(float x) {
  auto s = __builtin_amdgcn_permlane16_swap(bf(x), bf(x), false, false);
  x = fb(s[0]) + fb(s[1]);
  auto t = __builtin_amdgcn_permlane32_swap(bf(x), bf(x), false, false);
  return fb(t[0]) + fb(t[1]);
}
__device__ __forceinline__ float rsum4(f4 d) {
  auto s02 = __builtin_amdgcn_permlane32_swap(bf(d[0]), bf(d[2]), false, false);
  auto s13 = __builtin_amdgcn_permlane32_swap(bf(d[1]), bf(d[3]), false, false);
  const float x02 = fb(s02[0]) + fb(s02[1]), x13 = fb(s13[0]) + fb(s13[1]);
  auto s = __builtin_amdgcn_permlane16_swap(bf(x02), bf(x13), false, false);
  return fb(s[0]) + fb(s[1]);
}
template <int CTRL> __device__ __forceinline__ float dppf(float x) {
  return __builtin_bit_cast(float, __builtin_amdgcn_mov_dpp(__builtin_bit_cast(int, x), CTRL, 0xf, 0xf, true));
}
__device__ __forceinline__ u64 stale(v4u r, u32 tag) { return __builtin_amdgcn_ballot_w64(((r.y ^ tag) | (r.w ^ tag)) != 0u); }

__device__ __forceinline__ void poll2(v4u (&r)[2], u64 m0, u64 m1, const u8* base, u32 voff) {
  u64 sv;
  asm volatile("s_mov_b64 %2, exec\n\t"
               "s_mov_b64 exec, %5\n\tglobal_load_dwordx4 %0, %3, %4 sc1\n\t"
               "s_mov_b64 exec, %6\n\tglobal_load_dwordx4 %1, %3, %4 offset:1024 sc1\n\t"
               "s_mov_b64 exec, %2\n\ts_waitcnt vmcnt(0)"
               : "+v"(r[0]), "+v"(r[1]), "=&s"(sv) : "v"(voff), "s"(base), "s"(m0), "s"(m1) : "memory");
}
__device__ __forceinline__ void fetch_o(v4u (&r)[8], u32 want, const u8* base, u32 v0, u32 v1) {
  asm volatile("s_bitcmp1_b32 %11, 0\n\ts_cbranch_scc0 1f\n\t"
               "global_load_dwordx4 %0, %8, %10 sc1\n\tglobal_load_dwordx4 %1, %8, %10 offset:1024 sc1\n"
               "1:\n\ts_bitcmp1_b32 %11, 1\n\ts_cbranch_scc0 2f\n\t"
               "global_load_dwordx4 %2, %8, %10 offset:2048 sc1\n\tglobal_load_dwordx4 %3, %8, %10 offset:3072 sc1\n"
               "2:\n\ts_bitcmp1_b32 %11, 2\n\ts_cbranch_scc0 3f\n\t"
               "global_load_dwordx4 %4, %9, %10 sc1\n\tglobal_load_dwordx4 %5, %9, %10 offset:1024 sc1\n"
               "3:\n\ts_bitcmp1_b32 %11, 3\n\ts_cbranch_scc0 4f\n\t"
               "global_load_dwordx4 %6, %9, %10 offset:2048 sc1\n\tglobal_load_dwordx4 %7, %9, %10 offset:3072 sc1\n"
               "4:\n\ts_waitcnt vmcnt(0)"
               : "+v"(r[0]), "+v"(r[1]), "+v"(r[2]), "+v"(r[3]), "+v"(r[4]), "+v"(r[5]), "+v"(r[6]), "+v"(r[7])
               : "v"(v0), "v"(v1), "s"(base), "s"(want) : "memory", "scc");
}
__device__ __forceinline__ void fetch8(v4u (&r)[8], u32 want, const u8* base, u32 v0, u32 v1) {
  asm volatile("s_bitcmp1_b32 %11, 0\n\ts_cbranch_scc0 1f\n\tglobal_load_dwordx4 %0, %8, %10 sc1\n"
               "1:\n\ts_bitcmp1_b32 %11, 1\n\ts_cbranch_scc0 2f\n\tglobal_load_dwordx4 %1, %8, %10 offset:1024 sc1\n"
               "2:\n\ts_bitcmp1_b32 %11, 2\n\ts_cbranch_scc0 3f\n\tglobal_load_dwordx4 %2, %8, %10 offset:2048 sc1\n"
               "3:\n\ts_bitcmp1_b32 %11, 3\n\ts_cbranch_scc0 4f\n\tglobal_load_dwordx4 %3, %8, %10 offset:3072 sc1\n"
               "4:\n\ts_bitcmp1_b32 %11, 4\n\ts_cbranch_scc0 5f\n\tglobal_load_dwordx4 %4, %9, %10 sc1\n"
               "5:\n\ts_bitcmp1_b32 %11, 5\n\ts_cbranch_scc0 6f\n\tglobal_load_dwordx4 %5, %9, %10 offset:1024 sc1\n"
               "6:\n\ts_bitcmp1_b32 %11, 6\n\ts_cbranch_scc0 7f\n\tglobal_load_dwordx4 %6, %9, %10 offset:2048 sc1\n"
               "7:\n\ts_bitcmp1_b32 %11, 7\n\ts_cbranch_scc0 8f\n\tglobal_load_dwordx4 %7, %9, %10 offset:3072 sc1\n"
               "8:\n\ts_waitcnt vmcnt(0)"
               : "+v"(r[0]), "+v"(r[1]), "+v"(r[2]), "+v"(r[3]), "+v"(r[4]), "+v"(r[5]), "+v"(r[6]), "+v"(r[7])
               : "v"(v0), "v"(v1), "s"(base), "s"(want) : "memory", "scc");
}
__device__ __forceinline__ void ld8_issue(v4u (&r)[8], const u8* base, u32 v0, u32 v1) {
  asm volatile("global_load_dwordx4 %0, %8, %10 sc1\n\tglobal_load_dwordx4 %1, %8, %10 offset:1024 sc1\n\t"
               "global_load_dwordx4 %2, %8, %10 offset:2048 sc1\n\tglobal_load_dwordx4 %3, %8, %10 offset:3072 sc1\n\t"
               "global_load_dwordx4 %4, %9, %10 sc1\n\tglobal_load_dwordx4 %5, %9, %10 offset:1024 sc1\n\t"
               "global_load_dwordx4 %6, %9, %10 offset:2048 sc1\n\tglobal_load_dwordx4 %7, %9, %10 offset:3072 sc1"
               : "=&v"(r[0]), "=&v"(r[1]), "=&v"(r[2]), "=&v"(r[3]), "=&v"(r[4]), "=&v"(r[5]), "=&v"(r[6]), "=&v"(r[7])
               : "v"(v0), "v"(v1), "s"(base) : "memory");
}
__device__ __forceinline__ void ld8_wait(v4u (&r)[8]) {
  asm volatile("s_waitcnt vmcnt(0)"
               : "+v"(r[0]), "+v"(r[1]), "+v"(r[2]), "+v"(r[3]), "+v"(r[4]), "+v"(r[5]), "+v"(r[6]), "+v"(r[7]) :: "memory");
}
__device__ __forceinline__ void ld1(v4u& r, const u8* base, u32 v0) {
  asm volatile("global_load_dwordx4 %0, %1, %2 sc1\n\ts_waitcnt vmcnt(0)" : "=&v"(r) : "v"(v0), "s"(base) : "memory");
}
#define CBAR() asm volatile("" ::: "memory")
#define SBAR() __builtin_amdgcn_sched_barrier(0)
constexpr int thr_of(int n) { return TA + (TB - TA) * n / 63; }
template <int N> __device__ __forceinline__ void waitvm() { asm volatile("s_waitcnt vmcnt(%0)" ::"i"(N) : "memory"); }

__device__ __forceinline__ void flag_set(LAS u8* L, int f, int w, int lane) {
  CBAR();
  if (lane == 0) *(volatile LAS u32*)(L + L_F + f * 16 + w * 4) = 1u;
  CBAR();
}
template <int SLEEP> __device__ __forceinline__ void flag_wait4(LAS u8* L, int f) {
#pragma nounroll
  for (int i = 0; i < SPIN_L; ++i) {
    v4u v = *(volatile LAS v4u*)(L + L_F + f * 16);
    if (__builtin_amdgcn_readfirstlane(v.x & v.y & v.z & v.w)) break;
    if (SLEEP) __builtin_amdgcn_s_sleep(SLEEP);
  }
  CBAR();
}
template <int SLEEP> __device__ __forceinline__ void flag_wait1(LAS u8* L, int f, int w) {
#pragma nounroll
  for (int i = 0; i < SPIN_L; ++i) {
    u32 v = *(volatile LAS u32*)(L + L_F + f * 16 + w * 4);
    if (__builtin_amdgcn_readfirstlane(v)) break;
    if (SLEEP) __builtin_amdgcn_s_sleep(SLEEP);
  }
  CBAR();
}
#define STAMP(i) do { SBAR(); stamp[i] = __builtin_amdgcn_s_memrealtime(); SBAR(); } while (0)
#define STAMP_OUT() do { if (lane == 0) { for (int i_ = 0; i_ < 12; ++i_) ((GAS u64*)(ws + WS_STAMP))[(blockIdx.x * 8 + wave) * 16 + i_] = stamp[i_]; } } while (0)
#define MKRSRC(p, n) __builtin_amdgcn_make_buffer_rsrc((void*)(p), 0, (int)(n), 0x00020000)

__global__ void __launch_bounds__(512, 2)
gqa_fused(const u8* x, const u8* kc, const u8* vc, const u8* wq, const u8* wo, u8* out, u8* ws) {
  extern __shared__ __attribute__((aligned(16))) u8 smem[];
  LAS u8* L = (LAS u8*)smem;
  const int tid = threadIdx.x, lane = tid & 63, wave = __builtin_amdgcn_readfirstlane(tid >> 6);
  const int g = lane >> 4, c = lane & 15;
  const int bid = blockIdx.x, kvh = bid & 7, j = bid >> 3, b = j >> 2, split = j & 3;
  const int wg = kvh * 32 + j;
  const int bh = j >> 4, hl = (j >> 2) & 3, nq = j & 3;
  const __amdgpu_buffer_rsrc_t rs = MKRSRC(ws, WS_END);
  u64 stamp[12];
  for (int i = 0; i < 12; ++i) stamp[i] = 0;
  STAMP(0);

  if (wave < 4) {
    const int w = wave;
    const int r8 = (w + 4 * (j & 1) + (j >> 1)) & 7;
    const size_t kvoff = ((size_t)((b * 8 + kvh) * 2048 + split * 512 + w * 128)) * 256;
    const u8* kb = kc + kvoff;
    const __amdgpu_buffer_rsrc_t rv = MKRSRC(vc + kvoff, 32768);
    u32 ko[4];
#pragma unroll
    for (int i = 0; i < 4; ++i) ko[i] = g * 256 + ((c ^ (4 * g + i)) * 16);
    const u32 ldsk = (u32)(size_t)L + L_K + w * 32768;
#pragma unroll
    for (int q4 = 0; q4 < 8; ++q4) {
#pragma unroll
      for (int i = 0; i < 4; ++i) {
        const int n = q4 * 4 + i;
        if (n >= thr_of(n)) asm volatile("s_waitcnt vmcnt(%0)" ::"i"(thr_of(q4 * 4 + i) - 1) : "memory");
        u32 keep;
        asm volatile("s_mov_b32 %0, m0\n\ts_mov_b32 m0, %3\n\ts_nop 0\n\tglobal_load_lds_dwordx4 %1, %2 offset:%4 nt\n\ts_mov_b32 m0, %0"
                     : "=&s"(keep) : "v"(ko[i]), "s"(kb + ((q4 + r8) & 7) * 4096), "s"(ldsk + q4 * 4096), "i"(i * 1024) : "memory");
        if (n == QTRIG) flag_set(L, F_K2, w, lane);
        if (n == LFIRST - 1) {
          if (w == 0) {
            *(volatile LAS u32*)(L + L_F + lane * 4) = 0u;
            asm volatile("s_waitcnt lgkmcnt(0)" ::: "memory");
          }
          SBAR();
          __builtin_amdgcn_s_barrier();
          SBAR();
        }
      }
    }
    v4u v[32];
#pragma unroll
    for (int i = 0; i < 32; ++i) {
      asm volatile("s_waitcnt vmcnt(%0)" ::"i"(thr_of(32 + i) - 1) : "memory");
      if (i >= thr_of(32 + i) - 1 && (i == 0 || i - 1 < thr_of(31 + i) - 1)) { flag_set(L, F_K, w, lane); STAMP(1); }
      if (i == V2AT) flag_set(L, F_V2, w, lane);
      v[i] = __builtin_amdgcn_raw_buffer_load_b128(rv, lane * 16 + (i & 3) * 1024, (((i >> 2) + r8) & 7) * 4096, 2);
    }
    SBAR();
    flag_set(L, F_V, w, lane);
    STAMP(2);
    flag_wait4<1>(L, F_P);
    __builtin_amdgcn_s_setprio(3);
    v4u p[4];
#pragma unroll
    for (int t = 0; t < 4; ++t) p[t] = *(LAS v4u*)(L + L_P + w * 4096 + t * 1024 + lane * 16);
    const u32 tag = *(LAS u32*)(L + L_T);
    v4u stv;
    {
      const f4 mm = *(LAS f4*)(L + L_M + (4 * w + (lane & 3)) * 16), ll = *(LAS f4*)(L + L_L + (4 * w + (lane & 3)) * 16);
      stv = v4u{bf(fmaxf(fmaxf(mm[0], mm[1]), fmaxf(mm[2], mm[3])) * SCL), tag, bf((ll[0] + ll[1]) + (ll[2] + ll[3])), tag};
    }
    STAMP(6);
    f4 o[8];
#pragma unroll
    for (int e = 0; e < 8; ++e) o[e] = f4{0.f, 0.f, 0.f, 0.f};
#pragma unroll
    for (int t = 0; t < 4; ++t) {
#pragma unroll
      for (int e = 0; e < 8; ++e) o[e] = mfma(tr8(&v[8 * t], e), p[t], o[e]);
      SBAR();
    }
    STAMP(3);
#pragma unroll
    for (int e = 0; e < 8; ++e) *(LAS f4*)(L + L_K + w * 32768 + e * OSTR + lane * 16) = o[e];
    if (NOTAIL) { STAMP_OUT(); return; }
    flag_set(L, F_O, w, lane);
    const u32 obase = WS_BOX + (kvh * 32 + b * 4 + w) * BOX + 8192;
    flag_wait4<0>(L, F_O);
    {
      LAS u8* src = L + L_K + ((lane >> 2) & 7) * OSTR + ((lane >> 5) * 16 + 4 * w + (lane & 3)) * 16;
      f4 s0 = *(LAS f4*)src, s1 = *(LAS f4*)(src + 512);
#pragma unroll
      for (int ww = 1; ww < 4; ++ww) {
        s0 += *(LAS f4*)(src + ww * 32768);
        s1 += *(LAS f4*)(src + ww * 32768 + 512);
      }
      const u32 off = obase + (lane >> 5) * 2048 + ((lane >> 2) & 1) * 1024 + (((lane >> 3) & 3) * 16 + 4 * split + (lane & 3)) * 16;
      __builtin_amdgcn_raw_buffer_store_b128(v4u{pk(s0[0], s0[1]), tag, pk(s0[2], s0[3]), tag}, rs, off, 0, 16);
      __builtin_amdgcn_raw_buffer_store_b128(v4u{pk(s1[0], s1[1]), tag, pk(s1[2], s1[3]), tag}, rs, off + 4096, 0, 16);
      SBAR();
      if (lane < 4) __builtin_amdgcn_raw_buffer_store_b128(stv, rs, obase + 8192 + split * HSTR + lane * 16, 0, 16);
    }
    STAMP(4);
    if (w == 0) {
      __builtin_amdgcn_s_setprio(0);
      const u32 base = WS_BOX + wg * BOX + lane * 16;
      v4u r[8];
#pragma unroll
      for (int k = 0; k < 8; ++k) r[k] = v4u{0u, 0u, 0u, 0u};
      u32 todo = 255u;
      flag_wait1<2>(L, F_A, (kvh * 4 + hl) >> 3);
      __builtin_amdgcn_s_sleep(FDELAY);
#pragma nounroll
      for (int it = 0; it < SPIN_G; ++it) {
        fetch8(r, todo, ws, base, base + 4096);
        u32 nt = 0;
#pragma unroll
        for (int k = 0; k < 8; ++k) nt |= stale(r[k], tag) ? (1u << k) : 0u;
        todo = nt;
        if (!todo) break;
      }
      f4 s = {0.f, 0.f, 0.f, 0.f};
#pragma unroll
      for (int k = 0; k < 8; ++k) s += f4{fb(r[k].x << 16), fb(r[k].x & 0xFFFF0000u), fb(r[k].z << 16), fb(r[k].z & 0xFFFF0000u)};
      const float tot = rsum4(s);
      const int u = kvh * 4 + hl, row16 = u >> 1, eh = u & 1;
      const __bf16 ob = (__bf16)tot;
      *(GAS unsigned short*)(out + ((size_t)((bh * 16 + row16) * 512 + nq * 128 + 8 * c + 4 * eh + g)) * 2) = __builtin_bit_cast(unsigned short, ob);
      if ((bid & 127) == 0 && lane == 0) __builtin_amdgcn_raw_buffer_store_b32(tag, rs, WS_EPOCH + bh * 256, 0, 16);
      STAMP(5);
    }
    STAMP_OUT();
    return;
  }

  const int cw = wave - 4, ct = tid - 256;
  __builtin_amdgcn_s_setprio(3);
  const int qi = (j ^ QIX) & 7;
  const int qrot = WROT ? (j >> 3) * 4 : 0;
  u32 tag;
  v4u wv[16];
  {
    v4u xa[2];
    const u32 ep = __builtin_amdgcn_raw_buffer_load_b32(rs, ct == 0 ? WS_EPOCH + bh * 256 : 0x7FFFFFF0u, 0, 16);
    const __amdgpu_buffer_rsrc_t rx = MKRSRC(x, 32768);
    const __amdgpu_buffer_rsrc_t rq = MKRSRC(wq, NOWQ ? 0 : (4u << 20));
    const u32 xo = (((b & ~1) * 4 + (c & 7)) * 512 + cw * 128 + (2 * g + (c >> 3)) * 8) * 2;
#pragma unroll
    for (int T = 0; T < 2; ++T) xa[T] = __builtin_amdgcn_raw_buffer_load_b128(rx, xo, (T ^ (qrot >> 3)) * 128, 0);
    SBAR();
    const u32 wqo = ((cw * 128 + (lane >> 3) * 8) * 4096 + kvh * 512 + qi * 64 + (lane & 7) * 8) * 2;
#pragma unroll
    for (int i = 0; i < 16; ++i) {
      if (CTHR && i >= CTHR) waitvm<CTHR ? CTHR - 1 : 0>();
      const int ii = i ^ qrot;
      wv[i] = __builtin_amdgcn_raw_buffer_load_b128(rq, wqo, ((ii >> 3) * 64 + (ii & 7)) * 8192, 2);
      if (i == CFIRST - 1) { SBAR(); __builtin_amdgcn_s_barrier(); SBAR(); }
      if (CPAUSE >= 0 && i == CPAT - 1) waitvm<CPAUSE >= 0 ? CPAUSE : 0>();
    }
    SBAR();
    STAMP(11);
    if (qrot & 4) {
#pragma unroll
      for (int T = 0; T < 2; ++T) xa[T] = v4u{xa[T].z, xa[T].w, xa[T].x, xa[T].y};
    }
    f4 qa[8];
#pragma unroll
    for (int e = 0; e < 8; ++e) qa[e] = f4{0.f, 0.f, 0.f, 0.f};
#pragma unroll
    for (int T = 0; T < 2; ++T)
#pragma unroll
      for (int e = 0; e < 8; ++e) qa[e] = mfma(xa[T], tr8(&wv[8 * T], e), qa[e]);
    if ((g >> 1) == (c >> 3)) {
#pragma unroll
      for (int r = 0; r < 4; ++r) {
        LAS u8* dst = L + L_QP + cw * 4096 + (g * 4 + r) * 256 + (c & 7) * 32;
        *(LAS f4*)dst = f4{qa[0][r], qa[1][r], qa[2][r], qa[3][r]};
        *(LAS f4*)(dst + 16) = f4{qa[4][r], qa[5][r], qa[6][r], qa[7][r]};
      }
    }
    if (ct == 0) *(LAS u32*)(L + L_T) = ep + 1u;
    flag_set(L, F_QP, cw, lane);
    flag_wait4<0>(L, F_QP);
    tag = __builtin_amdgcn_readfirstlane(*(LAS u32*)(L + L_T));
    if (cw < 2 && !NOQPUB) {
      const int row = ct >> 4;
      f4 s = {0.f, 0.f, 0.f, 0.f};
#pragma unroll
      for (int ww = 0; ww < 8; ++ww) s += *(LAS f4*)(L + L_QP + ww * 2048 + row * 256 + c * 16);
      const u32 off = WS_QBUF + ((((b & ~1) + (row >> 2)) * 8 + kvh) * 16 + (qi >> 1) * 4 + (row & 3)) * 512 + (qi & 1) * 256 + c * 16;
      __builtin_amdgcn_raw_buffer_store_b128(v4u{pk(s[0], s[1]), tag, pk(s[2], s[3]), tag}, rs, off, 0, 16);
    }
    SBAR();
    STAMP(2);
  }
  {
    const __amdgpu_buffer_rsrc_t ro = MKRSRC(wo + (size_t)(kvh * 4 + hl) * 131072, NOWO ? 0 : 131072);
    const u32 woo = (cw * 32 + 2 * g) * 1024 + nq * 256 + c * 16;
#pragma unroll
    for (int i = 0; i < 8; ++i) wv[i] = __builtin_amdgcn_raw_buffer_load_b128(ro, woo, ((i & 3) * 8 + ((i >> 2) ^ (WROT ? bh : 0))) * 1024, 2);
    SBAR();
  }
  __builtin_amdgcn_s_sleep(QDELAY);
  {
    const u32 base = WS_QBUF + ((b * 8 + kvh) * 16 + cw * 4) * 512 + lane * 16;
    v4u r[2] = {v4u{0u, 0u, 0u, 0u}, v4u{0u, 0u, 0u, 0u}};
    u64 m0 = ~0ull, m1 = ~0ull;
#pragma nounroll
    for (int it = 0; it < (NOQPOLL ? 0 : NOQPUB ? 1 : SPIN_G); ++it) {
      poll2(r, m0, m1, ws, base);
      m0 = stale(r[0], tag);
      m1 = stale(r[1], tag);
      if (!(m0 | m1)) break;
    }
    LAS u8* dst = L + L_Q + (((lane >> 3) & 3) * 64 + ((lane >> 1) & 3) * 16 + cw * 4 + (lane >> 5)) * 16 + (lane & 1) * 8;
    *(LAS v2u*)dst = v2u{r[0].x, r[0].z};
    *(LAS v2u*)(dst + 32) = v2u{r[1].x, r[1].z};
    STAMP(3);
    flag_set(L, F_Q, cw, lane);
    flag_wait4<1>(L, F_Q);
  }
  v4u qf[4];
#pragma unroll
  for (int s = 0; s < 4; ++s) qf[s] = *(LAS v4u*)(L + L_Q + s * 1024 + lane * 16);
  flag_wait1<1>(L, F_K, cw);
  STAMP(4);
  f4 sc[8];
  {
    LAS u8* kr = L + L_K + cw * 32768 + (4 * (c & 3) + (c >> 2)) * 256;
    v4u kf[32];
#pragma unroll
    for (int u = 0; u < 8; ++u)
#pragma unroll
      for (int s = 0; s < 4; ++s) kf[u * 4 + s] = *(LAS v4u*)(kr + u * 4096 + ((4 * s + g) ^ c) * 16);
    SBAR();
#pragma unroll
    for (int u = 0; u < 8; ++u) {
      sc[u] = f4{0.f, 0.f, 0.f, 0.f};
#pragma unroll
      for (int s = 0; s < 4; ++s) sc[u] = mfma(kf[u * 4 + s], qf[s], sc[u]);
    }
  }
  {
    float m = sc[0][0];
#pragma unroll
    for (int u = 0; u < 8; ++u)
#pragma unroll
      for (int r = 0; r < 4; ++r) m = fmaxf(m, sc[u][r]);
    m = xmax(m);
    if (lane < 16) *(LAS float*)(L + L_M + c * 16 + cw * 4) = m;
    flag_set(L, F_M, cw, lane);
#pragma unroll
    for (int e = 0; e < 8; ++e) {
      const v4u fr = tr8(wv, e);
      LAS u8* d = L + L_K + cw * 32768 + 16384 + e * 1024 + lane * 16;
      *(LAS v2u*)(d + (WROT ? bh * 8 : 0)) = v2u{fr.x, fr.y};
      *(LAS v2u*)(d + (WROT ? 8 - bh * 8 : 8)) = v2u{fr.z, fr.w};
    }
    flag_wait4<0>(L, F_M);
    const f4 mm = *(LAS f4*)(L + L_M + c * 16);
    const float M = fmaxf(fmaxf(mm[0], mm[1]), fmaxf(mm[2], mm[3])) * SCL;
    float l = 0.f;
#pragma unroll
    for (int u = 0; u < 8; ++u)
#pragma unroll
      for (int r = 0; r < 4; ++r) {
        sc[u][r] = __builtin_amdgcn_exp2f(__builtin_fmaf(sc[u][r], SCL, -M));
        l += sc[u][r];
      }
    l = xsum(l);
    if (lane < 16) *(LAS float*)(L + L_L + c * 16 + cw * 4) = l;
#pragma unroll
    for (int t = 0; t < 4; ++t) {
      const v4u pv = {pk(sc[2 * t][0], sc[2 * t][1]), pk(sc[2 * t][2], sc[2 * t][3]), pk(sc[2 * t + 1][0], sc[2 * t + 1][1]), pk(sc[2 * t + 1][2], sc[2 * t + 1][3])};
      *(LAS v4u*)(L + L_P + cw * 4096 + t * 1024 + lane * 16) = pv;
    }
    flag_set(L, F_P, cw, lane);
    STAMP(5);
  }
  flag_set(L, F_W, cw, lane);
  __builtin_amdgcn_s_setprio(0);
  flag_wait4<1>(L, F_W);
  v4u wf[32];
#pragma unroll
  for (int t = 0; t < 4; ++t)
#pragma unroll
    for (int e = 0; e < 8; ++e) wf[t * 8 + e] = *(LAS v4u*)(L + L_K + t * 32768 + 16384 + e * 1024 + lane * 16);
#pragma unroll
  for (int i = 0; i < 32; ++i) asm volatile("" : "+v"(wf[i]));
  STAMP(6);
  if (NOTAIL) { STAMP_OUT(); return; }
  flag_wait4<2>(L, CSTART);
  STAMP(8);
  {
    const u32 obase = WS_BOX + (kvh * 32 + (bh * 4 + cw) * 4 + hl) * BOX + 8192;
    const u32 v0 = obase + lane * 16;
    const u32 vs = obase + 8192 + g * HSTR + (c & 3) * 16;
    const int u = (cw * 4 + g) * 2;
    const u32 off = WS_BOX + ((u >> 2) * 32 + bh * 16 + (u & 3) * 4 + nq) * BOX + (kvh * 4 + hl) * 256 + c * 16;
    v4u st;
#pragma nounroll
    for (int it = 0; it < SPIN_G; ++it) {
      ld1(st, ws, vs);
      if (!stale(st, tag)) break;
      __builtin_amdgcn_s_sleep(CSLEEP);
    }
    __builtin_amdgcn_s_setprio(3);
    if (HDELAY) __builtin_amdgcn_s_sleep(HDELAY);
    STAMP(7);
    float res[8];
#pragma nounroll
    for (int it = 0; it < SPIN_G; ++it) {
      v4u r[8];
      ld8_issue(r, ws, v0, v0 + 4096);
      SBAR();
      const float mx = xmax(fb(st.x));
      const float ex = __builtin_amdgcn_exp2f(fb(st.x) - mx);
      const float fq = ex * __builtin_amdgcn_rcpf(xsum(ex * fb(st.z)));
      f4 f = {dppf<0x00>(fq), dppf<0x55>(fq), dppf<0xAA>(fq), dppf<0xFF>(fq)};
      asm volatile("" : "+v"(f));
      ld8_wait(r);
      STAMP(9);
      v4u a[4];
#pragma unroll
      for (int t = 0; t < 4; ++t) a[t] = v4u{r[2 * t].x, r[2 * t].z, r[2 * t + 1].x, r[2 * t + 1].z};
      u32 bad = 0;
#pragma unroll
      for (int e = 0; e < 8; ++e) {
        f4 d = {0.f, 0.f, 0.f, 0.f};
#pragma unroll
        for (int t = 0; t < 4; ++t) d = mfma(a[t], wf[t * 8 + e], d);
        bad |= (r[e].y ^ tag) | (r[e].w ^ tag);
        res[e] = rsum4(d * f);
      }
      if (!__builtin_amdgcn_ballot_w64(bad != 0u)) break;
      stamp[1] = __builtin_amdgcn_s_memrealtime();
    }
    __builtin_amdgcn_raw_buffer_store_b128(v4u{pk(res[0], res[1]), tag, pk(res[2], res[3]), tag}, rs, off, 0, 16);
    __builtin_amdgcn_raw_buffer_store_b128(v4u{pk(res[4], res[5]), tag, pk(res[6], res[7]), tag}, rs, off + 4 * BOX, 0, 16);
    flag_set(L, F_A, cw, lane);
    STAMP(10);
  }
  STAMP_OUT();
}

__global__ void probe(const u8* ws) {
  __shared__ u64 smn[256], smx[256], ssum[256]; __shared__ int scnt[256];
  const u64* st = (const u64*)(ws + WS_STAMP);
  const int t = threadIdx.x;
  u64 mn = ~0ull;
  for (int w = 0; w < 8; ++w) { u64 v = st[(t * 8 + w) * 16]; if (v < mn) mn = v; }
  smn[t] = mn; __syncthreads();
  if (t == 0) { for (int i = 1; i < 256; ++i) if (smn[i] < smn[0]) smn[0] = smn[i]; }
  __syncthreads(); mn = smn[0]; __syncthreads();
  u64 lo = ~0ull, hi = 0, sum = 0; int n = 0;
  if (0) { for (int w = 0; w < 8; ++w) for (int i = 0; i < 12; ++i) { u64 v = st[(t * 8 + w) * 16 + i]; if (v && v - mn > hi) hi = v - mn; } }
  else for (int w = 0 * 4; w < 0 * 4 + 4; ++w) { u64 v = st[(t * 8 + w) * 16 + 0]; if (!v) continue; u64 d = v - mn; sum += d; n++; if (d < lo) lo = d; if (d > hi) hi = d; }
  smn[t] = lo; smx[t] = hi; ssum[t] = sum; scnt[t] = n; __syncthreads();
  if (t == 0) {
    for (int i = 1; i < 256; ++i) { if (smn[i] < smn[0]) smn[0] = smn[i]; if (smx[i] > smx[0]) smx[0] = smx[i]; ssum[0] += ssum[i]; scnt[0] += scnt[i]; }
    u64 val = smn[0];
    if (val > 20000) val = 20000;
    const u64 t0 = __builtin_amdgcn_s_memrealtime();
    while (__builtin_amdgcn_s_memrealtime() - t0 < val * 100) __builtin_amdgcn_s_sleep(8);
  }
}
extern "C" void kernel_launch(void* const* d_in, const int* in_sizes, int n_in, void* d_out, int out_size, void* d_ws,
                              size_t ws_size, hipStream_t stream) {
  (void)in_sizes; (void)n_in; (void)out_size; (void)ws_size;
  (void)hipFuncSetAttribute((const void*)gqa_fused, hipFuncAttributeMaxDynamicSharedMemorySize, (int)LDS_BYTES);
  hipLaunchKernelGGL(gqa_fused, dim3(256), dim3(512), LDS_BYTES, stream, (const u8*)d_in[0], (const u8*)d_in[1], (const u8*)d_in[2],
                     (const u8*)d_in[3], (const u8*)d_in[4], (u8*)d_out, (u8*)d_ws);
  probe<<<1, 256, 0, stream>>>((const u8*)d_ws);
}
```

```cpp
#define FDELAY 16
#define TA 7
#define TB 7
#include <hip/hip_runtime.h>
#include <stdint.h>

typedef unsigned u32;
typedef unsigned long long u64;
typedef unsigned char u8;
typedef u32 v4u __attribute__((ext_vector_type(4)));
typedef u32 v2u __attribute__((ext_vector_type(2)));
typedef float f4 __attribute__((ext_vector_type(4)));
typedef float f2 __attribute__((ext_vector_type(2)));
typedef short s8v __attribute__((ext_vector_type(8)));
typedef __bf16 bf2v __attribute__((ext_vector_type(2)));
#define GAS __attribute__((address_space(1)))
#define LAS __attribute__((address_space(3)))

#define LFIRST 4
#define CFIRST 8
#define QTRIG 28
#define CSTART F_V
#define CSLEEP 1
#define HINTPOS 1
#define HDELAY 0
#define HREP 1
#define L0ALL 1
#define STAMPS 0

constexpr u32 WS_EPOCH = 0;
constexpr u32 WS_QBUF = 4096;
constexpr u32 WS_BOX = WS_QBUF + 64 * 8192;
constexpr u32 BOX = 18432;
constexpr u32 WS_END = WS_BOX + 256 * BOX;
constexpr u32 WS_STAMP = 8u << 20;
constexpr u32 L_K = 0;
constexpr u32 L_P = 131072;
constexpr u32 L_QP = L_P, L_Q = L_P + 16384;
constexpr u32 L_M = L_Q + 4096, L_L = L_M + 256, L_T = L_L + 256, L_F = L_T + 64;
constexpr u32 LDS_BYTES = L_F + 256;
constexpr u32 OSTR = 1088;
enum { F_QP = 0, F_Q, F_K, F_M, F_P, F_O, F_V, F_A, F_K2 };

constexpr int SPIN_G = 1 << 17;
constexpr int SPIN_L = 1 << 22;
constexpr float SCL = 0.08838834764831845f * 1.4426950408889634f;

__device__ __forceinline__ f4 mfma(v4u a, v4u b, f4 c) {
  return __builtin_amdgcn_mfma_f32_16x16x32_bf16(__builtin_bit_cast(s8v, a), __builtin_bit_cast(s8v, b), c, 0, 0, 0);
}
__device__ __forceinline__ u32 pk(float lo, float hi) {
  f2 v = {lo, hi};
  return __builtin_bit_cast(u32, __builtin_convertvector(v, bf2v));
}
__device__ __forceinline__ u32 plo(u32 a, u32 b) { return __builtin_amdgcn_perm(b, a, 0x05040100u); }
__device__ __forceinline__ u32 phi(u32 a, u32 b) { return __builtin_amdgcn_perm(b, a, 0x07060302u); }
__device__ __forceinline__ v4u tr8(const v4u* v, int e) {
  v4u r;
#pragma unroll
  for (int jj = 0; jj < 4; ++jj) {
    const u32 a0 = v[2 * jj][e >> 1], a1 = v[2 * jj + 1][e >> 1];
    r[jj] = (e & 1) ? phi(a0, a1) : plo(a0, a1);
  }
  return r;
}
__device__ __forceinline__ float fb(u32 x) { return __builtin_bit_cast(float, x); }
__device__ __forceinline__ u32 bf(float x) { return __builtin_bit_cast(u32, x); }
__device__ __forceinline__ float xmax(float x) {
  auto s = __builtin_amdgcn_permlane16_swap(bf(x), bf(x), false, false);
  x = fmaxf(fb(s[0]), fb(s[1]));
  auto t = __builtin_amdgcn_permlane32_swap(bf(x), bf(x), false, false);
  return fmaxf(fb(t[0]), fb(t[1]));
}
__device__ __forceinline__ float xsum(float x) {
  auto s = __builtin_amdgcn_permlane16_swap(bf(x), bf(x), false, false);
  x = fb(s[0]) + fb(s[1]);
  auto t = __builtin_amdgcn_permlane32_swap(bf(x), bf(x), false, false);
  return fb(t[0]) + fb(t[1]);
}
__device__ __forceinline__ float rsum4(f4 d) {
  auto s02 = __builtin_amdgcn_permlane32_swap(bf(d[0]), bf(d[2]), false, false);
  auto s13 = __builtin_amdgcn_permlane32_swap(bf(d[1]), bf(d[3]), false, false);
  const float x02 = fb(s02[0]) + fb(s02[1]), x13 = fb(s13[0]) + fb(s13[1]);
  auto s = __builtin_amdgcn_permlane16_swap(bf(x02), bf(x13), false, false);
  return fb(s[0]) + fb(s[1]);
}
template <int CTRL> __device__ __forceinline__ float dppf(float x) {
  return __builtin_bit_cast(float, __builtin_amdgcn_mov_dpp(__builtin_bit_cast(int, x), CTRL, 0xf, 0xf, true));
}
__device__ __forceinline__ u64 stale(v4u r, u32 tag) { return __builtin_amdgcn_ballot_w64(((r.y ^ tag) | (r.w ^ tag)) != 0u); }

__device__ __forceinline__ void poll2(v4u (&r)[2], u64 m0, u64 m1, const u8* base, u32 voff) {
  u64 sv;
  asm volatile("s_mov_b64 %2, exec\n\t"
               "s_mov_b64 exec, %5\n\tglobal_load_dwordx4 %0, %3, %4 sc1\n\t"
               "s_mov_b64 exec, %6\n\tglobal_load_dwordx4 %1, %3, %4 offset:1024 sc1\n\t"
               "s_mov_b64 exec, %2\n\ts_waitcnt vmcnt(0)"
               : "+v"(r[0]), "+v"(r[1]), "=&s"(sv) : "v"(voff), "s"(base), "s"(m0), "s"(m1) : "memory");
}
__device__ __forceinline__ void ld8_issue(v4u (&r)[8], const u8* base, u32 v0, u32 v1) {
  asm volatile("global_load_dwordx4 %0, %8, %10 sc1\n\tglobal_load_dwordx4 %1, %8, %10 offset:1024 sc1\n\t"
               "global_load_dwordx4 %2, %8, %10 offset:2048 sc1\n\tglobal_load_dwordx4 %3, %8, %10 offset:3072 sc1\n\t"
               "global_load_dwordx4 %4, %9, %10 sc1\n\tglobal_load_dwordx4 %5, %9, %10 offset:1024 sc1\n\t"
               "global_load_dwordx4 %6, %9, %10 offset:2048 sc1\n\tglobal_load_dwordx4 %7, %9, %10 offset:3072 sc1"
               : "=&v"(r[0]), "=&v"(r[1]), "=&v"(r[2]), "=&v"(r[3]), "=&v"(r[4]), "=&v"(r[5]), "=&v"(r[6]), "=&v"(r[7])
               : "v"(v0), "v"(v1), "s"(base) : "memory");
}
__device__ __forceinline__ void ld8_wait(v4u (&r)[8]) {
  asm volatile("s_waitcnt vmcnt(0)"
               : "+v"(r[0]), "+v"(r[1]), "+v"(r[2]), "+v"(r[3]), "+v"(r[4]), "+v"(r[5]), "+v"(r[6]), "+v"(r[7]) :: "memory");
}
__device__ __forceinline__ u32 orxor(u32 acc, u32 a, u32 t) { return __builtin_amdgcn_bitop3_b32(acc, a, t, 0xF6); }
__device__ __forceinline__ void fetch8(v4u (&r)[8], u32 want, const u8* base, u32 v0, u32 v1) {
  asm volatile("s_bitcmp1_b32 %11, 0\n\ts_cbranch_scc0 1f\n\tglobal_load_dwordx4 %0, %8, %10 sc1\n"
               "1:\n\ts_bitcmp1_b32 %11, 1\n\ts_cbranch_scc0 2f\n\tglobal_load_dwordx4 %1, %8, %10 offset:1024 sc1\n"
               "2:\n\ts_bitcmp1_b32 %11, 2\n\ts_cbranch_scc0 3f\n\tglobal_load_dwordx4 %2, %8, %10 offset:2048 sc1\n"
               "3:\n\ts_bitcmp1_b32 %11, 3\n\ts_cbranch_scc0 4f\n\tglobal_load_dwordx4 %3, %8, %10 offset:3072 sc1\n"
               "4:\n\ts_bitcmp1_b32 %11, 4\n\ts_cbranch_scc0 5f\n\tglobal_load_dwordx4 %4, %9, %10 sc1\n"
               "5:\n\ts_bitcmp1_b32 %11, 5\n\ts_cbranch_scc0 6f\n\tglobal_load_dwordx4 %5, %9, %10 offset:1024 sc1\n"
               "6:\n\ts_bitcmp1_b32 %11, 6\n\ts_cbranch_scc0 7f\n\tglobal_load_dwordx4 %6, %9, %10 offset:2048 sc1\n"
               "7:\n\ts_bitcmp1_b32 %11, 7\n\ts_cbranch_scc0 8f\n\tglobal_load_dwordx4 %7, %9, %10 offset:3072 sc1\n"
               "8:\n\ts_waitcnt vmcnt(0)"
               : "+v"(r[0]), "+v"(r[1]), "+v"(r[2]), "+v"(r[3]), "+v"(r[4]), "+v"(r[5]), "+v"(r[6]), "+v"(r[7])
               : "v"(v0), "v"(v1), "s"(base), "s"(want) : "memory", "scc");
}
__device__ __forceinline__ void ld1(v4u& r, const u8* base, u32 v0) {
  asm volatile("global_load_dwordx4 %0, %1, %2 sc1\n\ts_waitcnt vmcnt(0)" : "=&v"(r) : "v"(v0), "s"(base) : "memory");
}
#define CBAR() asm volatile("" ::: "memory")
#define SBAR() __builtin_amdgcn_sched_barrier(0)
#define PIN(x) asm volatile("" : "+v"(x))
constexpr int thr_of(int n) { return TA + (TB - TA) * n / 63; }

__device__ __forceinline__ void flag_set(LAS u8* L, int f, int w, int lane) {
  CBAR();
  if (lane == 0) *(volatile LAS u32*)(L + L_F + f * 16 + w * 4) = 1u;
  CBAR();
}
template <int SLEEP> __device__ __forceinline__ void flag_wait4(LAS u8* L, int f) {
#pragma nounroll
  for (int i = 0; i < SPIN_L; ++i) {
    v4u v = *(volatile LAS v4u*)(L + L_F + f * 16);
    if (__builtin_amdgcn_readfirstlane(v.x & v.y & v.z & v.w)) break;
    if (SLEEP) __builtin_amdgcn_s_sleep(SLEEP);
  }
  CBAR();
}
template <int SLEEP> __device__ __forceinline__ void flag_wait1(LAS u8* L, int f, int w) {
#pragma nounroll
  for (int i = 0; i < SPIN_L; ++i) {
    u32 v = *(volatile LAS u32*)(L + L_F + f * 16 + w * 4);
    if (__builtin_amdgcn_readfirstlane(v)) break;
    if (SLEEP) __builtin_amdgcn_s_sleep(SLEEP);
  }
  CBAR();
}
#define STAMP(i) do { } while (0)
#define STAMP_OUT() do { } while (0)
#define MKRSRC(p, n) __builtin_amdgcn_make_buffer_rsrc((void*)(p), 0, (int)(n), 0x00020000)

__global__ void __launch_bounds__(512, 2)
gqa_fused(const u8* x, const u8* kc, const u8* vc, const u8* wq, const u8* wo, u8* out, u8* ws) {
  extern __shared__ __attribute__((aligned(16))) u8 smem[];
  LAS u8* L = (LAS u8*)smem;
  const int tid = threadIdx.x, lane = tid & 63, wave = __builtin_amdgcn_readfirstlane(tid >> 6);
  const int g = lane >> 4, c = lane & 15;
  const int bid = blockIdx.x, kvh = bid & 7, j = bid >> 3, b = j >> 2, split = j & 3;
  const int wg = kvh * 32 + j;
  const int bh = j >> 4, hl = (j >> 2) & 3, nq = j & 3;
  const __amdgpu_buffer_rsrc_t rs = MKRSRC(ws, WS_END);
  STAMP(0);

  if (wave < 4) {
    const int w = wave;
    const int r8 = (w + 4 * (j & 1) + (j >> 1)) & 7;
    const size_t kvoff = ((size_t)((b * 8 + kvh) * 2048 + split * 512 + w * 128)) * 256;
    const u8* kb = kc + kvoff;
    const __amdgpu_buffer_rsrc_t rv = MKRSRC(vc + kvoff, 32768);
    u32 ko[4];
#pragma unroll
    for (int i = 0; i < 4; ++i) ko[i] = g * 256 + ((c ^ (4 * g + i)) * 16);
    const u32 ldsk = (u32)(size_t)L + L_K + w * 32768;
#pragma unroll
    for (int q4 = 0; q4 < 8; ++q4) {
#pragma unroll
      for (int i = 0; i < 4; ++i) {
        const int n = q4 * 4 + i;
        if (n >= thr_of(n)) asm volatile("s_waitcnt vmcnt(%0)" ::"i"(thr_of(q4 * 4 + i) - 1) : "memory");
        u32 keep;
        asm volatile("s_mov_b32 %0, m0\n\ts_mov_b32 m0, %3\n\ts_nop 0\n\tglobal_load_lds_dwordx4 %1, %2 offset:%4 nt\n\ts_mov_b32 m0, %0"
                     : "=&s"(keep) : "v"(ko[i]), "s"(kb + ((q4 + r8) & 7) * 4096), "s"(ldsk + q4 * 4096), "i"(i * 1024) : "memory");
        if (n == QTRIG) flag_set(L, F_K2, w, lane);
        if (n == LFIRST - 1) {
          if (w == 0) {
            *(volatile LAS u32*)(L + L_F + lane * 4) = 0u;
            asm volatile("s_waitcnt lgkmcnt(0)" ::: "memory");
          }
          SBAR();
          __builtin_amdgcn_s_barrier();
          SBAR();
        }
      }
    }
    v4u v[32];
#pragma unroll
    for (int i = 0; i < 32; ++i) {
      asm volatile("s_waitcnt vmcnt(%0)" ::"i"(thr_of(32 + i) - 1) : "memory");
      if (i >= thr_of(32 + i) - 1 && (i == 0 || i - 1 < thr_of(31 + i) - 1)) { flag_set(L, F_K, w, lane); STAMP(1); }
      v[i] = __builtin_amdgcn_raw_buffer_load_b128(rv, lane * 16 + (i & 3) * 1024, (((i >> 2) + r8) & 7) * 4096, 2);
    }
    SBAR();
    flag_set(L, F_V, w, lane);
    STAMP(2);
    flag_wait4<1>(L, F_P);
    __builtin_amdgcn_s_setprio(3);
    v4u p[4];
#pragma unroll
    for (int t = 0; t < 4; ++t) p[t] = *(LAS v4u*)(L + L_P + w * 4096 + t * 1024 + lane * 16);
    const u32 tag = __builtin_amdgcn_readfirstlane(*(LAS u32*)(L + L_T));
    v4u stv;
    {
      const f4 mm = *(LAS f4*)(L + L_M + (4 * w + (lane & 3)) * 16), ll = *(LAS f4*)(L + L_L + (4 * w + (lane & 3)) * 16);
      stv = v4u{bf(fmaxf(fmaxf(mm[0], mm[1]), fmaxf(mm[2], mm[3])) * SCL), tag, bf((ll[0] + ll[1]) + (ll[2] + ll[3])), tag};
    }
    const u32 obase = WS_BOX + (kvh * 32 + b * 4 + w) * BOX + 8192;
    u32 ooff = obase + (lane >> 5) * 2048 + ((lane >> 2) & 1) * 1024 + (((lane >> 3) & 3) * 16 + 4 * split + (lane & 3)) * 16;
    u32 soff = lane < HREP * 4 ? obase + 8192 + (lane >> 2) * 512 + split * 128 + (lane & 3) * 16 : 0x7FFFFFF0u;
    u32 rsrc_ = L_K + ((lane >> 2) & 7) * OSTR + ((lane >> 5) * 16 + 4 * w + (lane & 3)) * 16;
    u32 wdst_ = L_K + w * 32768 + lane * 16;
    PIN(rsrc_); PIN(wdst_); PIN(ooff); PIN(soff); PIN(stv);
    STAMP(6);
    f4 o[8];
#pragma unroll
    for (int e = 0; e < 8; ++e) o[e] = f4{0.f, 0.f, 0.f, 0.f};
#pragma unroll
    for (int t = 0; t < 4; ++t) {
#pragma unroll
      for (int e = 0; e < 8; ++e) o[e] = mfma(tr8(&v[8 * t], e), p[t], o[e]);
      if (t < 3) SBAR();
    }
    STAMP(3);
#pragma unroll
    for (int e = 0; e < 8; ++e) *(LAS f4*)(L + wdst_ + e * OSTR) = o[e];
    flag_set(L, F_O, w, lane);
    flag_wait4<0>(L, F_O);
    {
      LAS u8* src = L + rsrc_;
      f4 s0 = *(LAS f4*)src, s1 = *(LAS f4*)(src + 512);
#pragma unroll
      for (int ww = 1; ww < 4; ++ww) {
        s0 += *(LAS f4*)(src + ww * 32768);
        s1 += *(LAS f4*)(src + ww * 32768 + 512);
      }
      __builtin_amdgcn_raw_buffer_store_b128(v4u{pk(s0[0], s0[1]), tag, pk(s0[2], s0[3]), tag}, rs, ooff, 0, 16);
      __builtin_amdgcn_raw_buffer_store_b128(v4u{pk(s1[0], s1[1]), tag, pk(s1[2], s1[3]), tag}, rs, ooff, 4096, 16);
      SBAR();
      __builtin_amdgcn_raw_buffer_store_b128(stv, rs, soff, 0, 16);
    }
    STAMP(4);
    if (w == 0) {
      __builtin_amdgcn_s_setprio(0);
      const u32 base = WS_BOX + wg * BOX + lane * 16;
      const int u = kvh * 4 + hl, row16 = u >> 1, eh = u & 1;
      u32 oo = ((bh * 16 + row16) * 512 + nq * 128 + 8 * c + 4 * eh + g) * 2;
      PIN(oo);
      v4u r[8];
#pragma unroll
      for (int k = 0; k < 8; ++k) r[k] = v4u{0u, 0u, 0u, 0u};
      u32 todo = 255u;
      flag_wait4<2>(L, F_A);
      __builtin_amdgcn_s_sleep(FDELAY);
#pragma nounroll
      for (int it = 0; it < SPIN_G; ++it) {
        fetch8(r, todo, ws, base, base + 4096);
        u32 nt = 0;
#pragma unroll
        for (int k = 0; k < 8; ++k) nt |= stale(r[k], tag) ? (1u << k) : 0u;
        todo = nt;
        if (!todo) break;
      }
      f4 s = {0.f, 0.f, 0.f, 0.f};
#pragma unroll
      for (int k = 0; k < 8; ++k) s += f4{fb(r[k].x << 16), fb(r[k].x & 0xFFFF0000u), fb(r[k].z << 16), fb(r[k].z & 0xFFFF0000u)};
      const float tot = rsum4(s);
      const __bf16 ob = (__bf16)tot;
      *(GAS unsigned short*)(out + oo) = __builtin_bit_cast(unsigned short, ob);
      if ((bid & 127) == 0 && lane == 0) __builtin_amdgcn_raw_buffer_store_b32(tag, rs, WS_EPOCH + bh * 256, 0, 16);
      STAMP(5);
    }
    STAMP_OUT();
    return;
  }

  const int cw = wave - 4, ct = tid - 256;
  __builtin_amdgcn_s_setprio(3);
  const int qi = j & 7;
  const int qrot = (j >> 3) * 4;
  u32 tag;
  v4u wv[16];
  {
    v4u xa[2];
    const u32 ep = __builtin_amdgcn_raw_buffer_load_b32(rs, ct == 0 ? WS_EPOCH + bh * 256 : 0x7FFFFFF0u, 0, 16);
    const __amdgpu_buffer_rsrc_t rx = MKRSRC(x, 32768);
    const __amdgpu_buffer_rsrc_t rq = MKRSRC(wq, 4u << 20);
    const u32 xo = (((b & ~1) * 4 + (c & 7)) * 512 + cw * 128 + (2 * g + (c >> 3)) * 8) * 2;
#pragma unroll
    for (int T = 0; T < 2; ++T) xa[T] = __builtin_amdgcn_raw_buffer_load_b128(rx, xo, (T ^ (qrot >> 3)) * 128, 0);
    SBAR();
    const u32 wqo = ((cw * 128 + (lane >> 3) * 8) * 4096 + kvh * 512 + qi * 64 + (lane & 7) * 8) * 2;
#pragma unroll
    for (int i = 0; i < 16; ++i) {
      const int ii = i ^ qrot;
      wv[i] = __builtin_amdgcn_raw_buffer_load_b128(rq, wqo, ((ii >> 3) * 64 + (ii & 7)) * 8192, 0);
      if (i == CFIRST - 1) { SBAR(); __builtin_amdgcn_s_barrier(); SBAR(); }
    }
    SBAR();
    STAMP(11);
    if (qrot & 4) {
#pragma unroll
      for (int T = 0; T < 2; ++T) xa[T] = v4u{xa[T].z, xa[T].w, xa[T].x, xa[T].y};
    }
    f4 qa[8];
#pragma unroll
    for (int e = 0; e < 8; ++e) qa[e] = f4{0.f, 0.f, 0.f, 0.f};
#pragma unroll
    for (int T = 0; T < 2; ++T)
#pragma unroll
      for (int e = 0; e < 8; ++e) qa[e] = mfma(xa[T], tr8(&wv[8 * T], e), qa[e]);
    if ((g >> 1) == (c >> 3)) {
#pragma unroll
      for (int r = 0; r < 4; ++r) {
        LAS u8* dst = L + L_QP + cw * 4096 + (g * 4 + r) * 256 + (c & 7) * 32;
        *(LAS f4*)dst = f4{qa[0][r], qa[1][r], qa[2][r], qa[3][r]};
        *(LAS f4*)(dst + 16) = f4{qa[4][r], qa[5][r], qa[6][r], qa[7][r]};
      }
    }
    if (ct == 0) *(LAS u32*)(L + L_T) = ep + 1u;
    flag_set(L, F_QP, cw, lane);
    flag_wait4<0>(L, F_QP);
    tag = __builtin_amdgcn_readfirstlane(*(LAS u32*)(L + L_T));
    if (cw < 2) {
      const int row = ct >> 4;
      f4 s = {0.f, 0.f, 0.f, 0.f};
#pragma unroll
      for (int ww = 0; ww < 8; ++ww) s += *(LAS f4*)(L + L_QP + ww * 2048 + row * 256 + c * 16);
      const u32 off = WS_QBUF + ((((b & ~1) + (row >> 2)) * 8 + kvh) * 16 + (qi >> 1) * 4 + (row & 3)) * 512 + (qi & 1) * 256 + c * 16;
      __builtin_amdgcn_raw_buffer_store_b128(v4u{pk(s[0], s[1]), tag, pk(s[2], s[3]), tag}, rs, off, 0, 16);
    }
    SBAR();
    STAMP(2);
  }
  {
    const __amdgpu_buffer_rsrc_t ro = MKRSRC(wo + (size_t)(kvh * 4 + hl) * 131072, 131072);
    const u32 woo = (cw * 32 + 2 * g) * 1024 + nq * 256 + c * 16;
#pragma unroll
    for (int i = 0; i < 8; ++i) wv[i] = __builtin_amdgcn_raw_buffer_load_b128(ro, woo, ((i & 3) * 8 + ((i >> 2) ^ bh)) * 1024, 0);
    SBAR();
  }
  flag_wait1<4>(L, F_K2, cw);
  {
    const u32 base = WS_QBUF + ((b * 8 + kvh) * 16 + cw * 4) * 512 + lane * 16;
    v4u r[2] = {v4u{0u, 0u, 0u, 0u}, v4u{0u, 0u, 0u, 0u}};
    u64 m0 = ~0ull, m1 = ~0ull;
#pragma nounroll
    for (int it = 0; it < SPIN_G; ++it) {
      poll2(r, m0, m1, ws, base);
      m0 = stale(r[0], tag);
      m1 = stale(r[1], tag);
      if (!(m0 | m1)) break;
    }
    LAS u8* dst = L + L_Q + (((lane >> 3) & 3) * 64 + ((lane >> 1) & 3) * 16 + cw * 4 + (lane >> 5)) * 16 + (lane & 1) * 8;
    *(LAS v2u*)dst = v2u{r[0].x, r[0].z};
    *(LAS v2u*)(dst + 32) = v2u{r[1].x, r[1].z};
    STAMP(3);
    flag_set(L, F_Q, cw, lane);
    flag_wait4<1>(L, F_Q);
  }
  v4u qf[4];
#pragma unroll
  for (int s = 0; s < 4; ++s) qf[s] = *(LAS v4u*)(L + L_Q + s * 1024 + lane * 16);
  flag_wait1<1>(L, F_K, cw);
  STAMP(4);
  f4 sc[8];
  {
    LAS u8* kr = L + L_K + cw * 32768 + (4 * (c & 3) + (c >> 2)) * 256;
    v4u kf[32];
#pragma unroll
    for (int u = 0; u < 8; ++u)
#pragma unroll
      for (int s = 0; s < 4; ++s) kf[u * 4 + s] = *(LAS v4u*)(kr + u * 4096 + ((4 * s + g) ^ c) * 16);
    SBAR();
#pragma unroll
    for (int u = 0; u < 8; ++u) {
      sc[u] = f4{0.f, 0.f, 0.f, 0.f};
#pragma unroll
      for (int s = 0; s < 4; ++s) sc[u] = mfma(kf[u * 4 + s], qf[s], sc[u]);
    }
  }
  {
    float m = sc[0][0];
#pragma unroll
    for (int u = 0; u < 8; ++u)
#pragma unroll
      for (int r = 0; r < 4; ++r) m = fmaxf(m, sc[u][r]);
    m = xmax(m);
    if (lane < 16) *(LAS float*)(L + L_M + c * 16 + cw * 4) = m;
    flag_set(L, F_M, cw, lane);
#pragma unroll
    for (int e = 0; e < 8; ++e) {
      const v4u fr = tr8(wv, e);
      LAS u8* d = L + L_K + cw * 32768 + 16384 + e * 1024 + lane * 16;
      *(LAS v4u*)d = bh ? v4u{fr.z, fr.x, fr.w, fr.y} : v4u{fr.x, fr.z, fr.y, fr.w};
    }
    flag_wait4<0>(L, F_M);
    const f4 mm = *(LAS f4*)(L + L_M + c * 16);
    const float M = fmaxf(fmaxf(mm[0], mm[1]), fmaxf(mm[2], mm[3])) * SCL;
    float l = 0.f;
#pragma unroll
    for (int u = 0; u < 8; ++u)
#pragma unroll
      for (int r = 0; r < 4; ++r) {
        sc[u][r] = __builtin_amdgcn_exp2f(__builtin_fmaf(sc[u][r], SCL, -M));
        l += sc[u][r];
      }
    l = xsum(l);
    if (lane < 16) *(LAS float*)(L + L_L + c * 16 + cw * 4) = l;
#pragma unroll
    for (int t = 0; t < 4; ++t) {
      const v4u pv = {pk(sc[2 * t][0], sc[2 * t][1]), pk(sc[2 * t][2], sc[2 * t][3]), pk(sc[2 * t + 1][0], sc[2 * t + 1][1]), pk(sc[2 * t + 1][2], sc[2 * t + 1][3])};
      *(LAS v4u*)(L + L_P + cw * 4096 + t * 1024 + lane * 16) = pv;
    }
    flag_set(L, F_P, cw, lane);
    STAMP(5);
  }
  __builtin_amdgcn_s_setprio(0);
  flag_wait4<1>(L, F_P);
  v4u wf[32];
#pragma unroll
  for (int t = 0; t < 4; ++t)
#pragma unroll
    for (int e = 0; e < 8; ++e) wf[t * 8 + e] = *(LAS v4u*)(L + L_K + t * 32768 + 16384 + e * 1024 + lane * 16);
#pragma unroll
  for (int i = 0; i < 32; ++i) PIN(wf[i]);
  STAMP(6);
  {
    const u32 obase = WS_BOX + (kvh * 32 + (bh * 4 + cw) * 4 + hl) * BOX + 8192;
    u32 v0 = obase + lane * 16, v1 = v0 + 4096;
    const u32 vs = obase + 8192 + (HREP > 1 ? nq * 512 : 0) + g * 128 + (c & 3) * 16;
    const int u = (cw * 4 + g) * 2;
    u32 off = WS_BOX + ((u >> 2) * 32 + bh * 16 + (u & 3) * 4 + nq) * BOX + (kvh * 4 + hl) * 256 + c * 16;
    PIN(off); PIN(v0); PIN(v1);
    flag_wait4<2>(L, CSTART);
    STAMP(8);
    v4u st;
#pragma nounroll
    for (int it = 0; it < SPIN_G; ++it) {
      ld1(st, ws, vs);
      if (!stale(st, tag)) break;
      if (CSLEEP) __builtin_amdgcn_s_sleep(CSLEEP);
    }
    __builtin_amdgcn_s_setprio(3);
    if (HDELAY) __builtin_amdgcn_s_sleep(HDELAY);
    STAMP(7);
    float res[8];
#pragma nounroll
    for (int it = 0; it < SPIN_G; ++it) {
      v4u r[8];
      ld8_issue(r, ws, v0, v1);
      SBAR();
      const float mx = xmax(fb(st.x));
      const float ex = __builtin_amdgcn_exp2f(fb(st.x) - mx);
      const float fq = ex * __builtin_amdgcn_rcpf(xsum(ex * fb(st.z)));
      f4 f = {dppf<0x00>(fq), dppf<0x55>(fq), dppf<0xAA>(fq), dppf<0xFF>(fq)};
      PIN(f);
      ld8_wait(r);
      STAMP(9);
      u32 bad = 0;
#pragma unroll
      for (int t = 0; t < 4; ++t) {
        bad = orxor(orxor(bad, r[2 * t].y, tag), r[2 * t].w, tag);
        r[2 * t].y = r[2 * t + 1].x;
        r[2 * t].w = r[2 * t + 1].z;
      }
#pragma unroll
      for (int e = 0; e < 8; ++e) {
        f4 d = {0.f, 0.f, 0.f, 0.f};
#pragma unroll
        for (int t = 0; t < 4; ++t) d = mfma(r[2 * t], wf[t * 8 + e], d);
        if (e < 4) bad = orxor(orxor(bad, r[2 * e + 1].y, tag), r[2 * e + 1].w, tag);
        res[e] = rsum4(d * f);
      }
      if (!__builtin_amdgcn_ballot_w64(bad != 0u)) break;
    }
    __builtin_amdgcn_raw_buffer_store_b128(v4u{pk(res[0], res[1]), tag, pk(res[2], res[3]), tag}, rs, off, 0, 16);
    __builtin_amdgcn_raw_buffer_store_b128(v4u{pk(res[4], res[5]), tag, pk(res[6], res[7]), tag}, rs, off, 4 * BOX, 16);
    flag_set(L, F_A, cw, lane);
    STAMP(10);
  }
  STAMP_OUT();
}

extern "C" void kernel_launch(void* const* d_in, const int* in_sizes, int n_in, void* d_out, int out_size, void* d_ws,
                              size_t ws_size, hipStream_t stream) {
  (void)in_sizes; (void)n_in; (void)out_size; (void)ws_size;
  (void)hipFuncSetAttribute((const void*)gqa_fused, hipFuncAttributeMaxDynamicSharedMemorySize, (int)LDS_BYTES);
  hipLaunchKernelGGL(gqa_fused, dim3(256), dim3(512), LDS_BYTES, stream, (const u8*)d_in[0], (const u8*)d_in[1], (const u8*)d_in[2],
                     (const u8*)d_in[3], (const u8*)d_in[4], (u8*)d_out, (u8*)d_ws);
}
```

```cpp
#define TA 11
#define TB 11
#define CODEKB 13
#define EARLYT 0
#define FDELAY 12
#define CODEPF 0
#include <hip/hip_runtime.h>
#include <stdint.h>

typedef unsigned u32;
typedef unsigned long long u64;
typedef unsigned char u8;
typedef u32 v4u __attribute__((ext_vector_type(4)));
typedef u32 v2u __attribute__((ext_vector_type(2)));
typedef float f4 __attribute__((ext_vector_type(4)));
typedef float f2 __attribute__((ext_vector_type(2)));
typedef short s8v __attribute__((ext_vector_type(8)));
typedef __bf16 bf2v __attribute__((ext_vector_type(2)));
#define GAS __attribute__((address_space(1)))
#define LAS __attribute__((address_space(3)))

#define LFIRST 4
#define CFIRST 8
#define QTRIG 28
#define CSTART F_V
#define CSLEEP 1
#define HINTPOS 1
#define HDELAY 0
#define HREP 1
#define L0ALL 1
#define PEND 0
#define PDELTA 142
#define PSTEP 2
#define PMIN 16
#define CODEBACK 0
#define OUTAUX -1
#define FSLEEP 0
#define STAMPS 0

constexpr u32 WS_EPOCH = 0;
constexpr u32 WS_QBUF = 4096;
constexpr u32 WS_BOX = WS_QBUF + 64 * 8192;
constexpr u32 BOX = 18432;
constexpr u32 WS_END = WS_BOX + 256 * BOX;
constexpr u32 WS_STAMP = 8u << 20;
constexpr u32 L_K = 0;
constexpr u32 L_P = 131072;
constexpr u32 L_QP = L_P, L_Q = L_P + 16384;
constexpr u32 L_M = L_Q + 4096, L_L = L_M + 256, L_T = L_L + 256, L_F = L_T + 64;
constexpr u32 LDS_BYTES = L_F + 256;
enum { F_QP = 0, F_Q, F_K, F_M, F_P, F_O, F_V, F_A, F_K2 };

constexpr int SPIN_G = 1 << 17;
constexpr int SPIN_L = 1 << 22;
constexpr float SCL = 0.08838834764831845f * 1.4426950408889634f;

__device__ __forceinline__ f4 mfma(v4u a, v4u b, f4 c) {
  return __builtin_amdgcn_mfma_f32_16x16x32_bf16(__builtin_bit_cast(s8v, a), __builtin_bit_cast(s8v, b), c, 0, 0, 0);
}
__device__ __forceinline__ u32 pk(float lo, float hi) {
  f2 v = {lo, hi};
  return __builtin_bit_cast(u32, __builtin_convertvector(v, bf2v));
}
__device__ __forceinline__ u32 plo(u32 a, u32 b) { return __builtin_amdgcn_perm(b, a, 0x05040100u); }
__device__ __forceinline__ u32 phi(u32 a, u32 b) { return __builtin_amdgcn_perm(b, a, 0x07060302u); }
__device__ __forceinline__ v4u tr8(const v4u* v, int e) {
  v4u r;
#pragma unroll
  for (int jj = 0; jj < 4; ++jj) {
    const u32 a0 = v[2 * jj][e >> 1], a1 = v[2 * jj + 1][e >> 1];
    r[jj] = (e & 1) ? phi(a0, a1) : plo(a0, a1);
  }
  return r;
}
__device__ __forceinline__ float fb(u32 x) { return __builtin_bit_cast(float, x); }
__device__ __forceinline__ u32 bf(float x) { return __builtin_bit_cast(u32, x); }
__device__ __forceinline__ float xmax(float x) {
  auto s = __builtin_amdgcn_permlane16_swap(bf(x), bf(x), false, false);
  x = fmaxf(fb(s[0]), fb(s[1]));
  auto t = __builtin_amdgcn_permlane32_swap(bf(x), bf(x), false, false);
  return fmaxf(fb(t[0]), fb(t[1]));
}
__device__ __forceinline__ float xsum(float x) {
  auto s = __builtin_amdgcn_permlane16_swap(bf(x), bf(x), false, false);
  x = fb(s[0]) + fb(s[1]);
  auto t = __builtin_amdgcn_permlane32_swap(bf(x), bf(x), false, false);
  return fb(t[0]) + fb(t[1]);
}
__device__ __forceinline__ float rsum4(f4 d) {
  auto s02 = __builtin_amdgcn_permlane32_swap(bf(d[0]), bf(d[2]), false, false);
  auto s13 = __builtin_amdgcn_permlane32_swap(bf(d[1]), bf(d[3]), false, false);
  const float x02 = fb(s02[0]) + fb(s02[1]), x13 = fb(s13[0]) + fb(s13[1]);
  auto s = __builtin_amdgcn_permlane16_swap(bf(x02), bf(x13), false, false);
  return fb(s[0]) + fb(s[1]);
}
template <int CTRL> __device__ __forceinline__ float dppf(float x) {
  return __builtin_bit_cast(float, __builtin_amdgcn_mov_dpp(__builtin_bit_cast(int, x), CTRL, 0xf, 0xf, true));
}
__device__ __forceinline__ u64 stale(v4u r, u32 tag) { return __builtin_amdgcn_ballot_w64(((r.y ^ tag) | (r.w ^ tag)) != 0u); }

__device__ __forceinline__ void poll2(v4u (&r)[2], u64 m0, u64 m1, const u8* base, u32 voff) {
  u64 sv;
  asm volatile("s_mov_b64 %2, exec\n\t"
               "s_mov_b64 exec, %5\n\tglobal_load_dwordx4 %0, %3, %4 sc1\n\t"
               "s_mov_b64 exec, %6\n\tglobal_load_dwordx4 %1, %3, %4 offset:1024 sc1\n\t"
               "s_mov_b64 exec, %2\n\ts_waitcnt vmcnt(0)"
               : "+v"(r[0]), "+v"(r[1]), "=&s"(sv) : "v"(voff), "s"(base), "s"(m0), "s"(m1) : "memory");
}
__device__ __forceinline__ void ld8_issue(v4u (&r)[8], const u8* base, u32 v0) {
  asm volatile("global_load_dwordx4 %0, %8, %9 sc1\n\tglobal_load_dwordx4 %1, %8, %9 offset:256 sc1\n\t"
               "global_load_dwordx4 %2, %8, %9 offset:512 sc1\n\tglobal_load_dwordx4 %3, %8, %9 offset:768 sc1\n\t"
               "global_load_dwordx4 %4, %8, %9 offset:1024 sc1\n\tglobal_load_dwordx4 %5, %8, %9 offset:1280 sc1\n\t"
               "global_load_dwordx4 %6, %8, %9 offset:1536 sc1\n\tglobal_load_dwordx4 %7, %8, %9 offset:1792 sc1"
               : "=&v"(r[0]), "=&v"(r[1]), "=&v"(r[2]), "=&v"(r[3]), "=&v"(r[4]), "=&v"(r[5]), "=&v"(r[6]), "=&v"(r[7])
               : "v"(v0), "s"(base) : "memory");
}
__device__ __forceinline__ void ld8_wait(v4u (&r)[8]) {
  asm volatile("s_waitcnt vmcnt(0)"
               : "+v"(r[0]), "+v"(r[1]), "+v"(r[2]), "+v"(r[3]), "+v"(r[4]), "+v"(r[5]), "+v"(r[6]), "+v"(r[7]) :: "memory");
}
__device__ __forceinline__ u32 orxor(u32 acc, u32 a, u32 t) { return __builtin_amdgcn_bitop3_b32(acc, a, t, 0xF6); }
__device__ __forceinline__ void fetch8(v4u (&r)[8], u32 want, const u8* base, u32 v0, u32 v1) {
  asm volatile("s_bitcmp1_b32 %11, 0\n\ts_cbranch_scc0 1f\n\tglobal_load_dwordx4 %0, %8, %10 sc1\n"
               "1:\n\ts_bitcmp1_b32 %11, 1\n\ts_cbranch_scc0 2f\n\tglobal_load_dwordx4 %1, %8, %10 offset:1024 sc1\n"
               "2:\n\ts_bitcmp1_b32 %11, 2\n\ts_cbranch_scc0 3f\n\tglobal_load_dwordx4 %2, %8, %10 offset:2048 sc1\n"
               "3:\n\ts_bitcmp1_b32 %11, 3\n\ts_cbranch_scc0 4f\n\tglobal_load_dwordx4 %3, %8, %10 offset:3072 sc1\n"
               "4:\n\ts_bitcmp1_b32 %11, 4\n\ts_cbranch_scc0 5f\n\tglobal_load_dwordx4 %4, %9, %10 sc1\n"
               "5:\n\ts_bitcmp1_b32 %11, 5\n\ts_cbranch_scc0 6f\n\tglobal_load_dwordx4 %5, %9, %10 offset:1024 sc1\n"
               "6:\n\ts_bitcmp1_b32 %11, 6\n\ts_cbranch_scc0 7f\n\tglobal_load_dwordx4 %6, %9, %10 offset:2048 sc1\n"
               "7:\n\ts_bitcmp1_b32 %11, 7\n\ts_cbranch_scc0 8f\n\tglobal_load_dwordx4 %7, %9, %10 offset:3072 sc1\n"
               "8:\n\ts_waitcnt vmcnt(0)"
               : "+v"(r[0]), "+v"(r[1]), "+v"(r[2]), "+v"(r[3]), "+v"(r[4]), "+v"(r[5]), "+v"(r[6]), "+v"(r[7])
               : "v"(v0), "v"(v1), "s"(base), "s"(want) : "memory", "scc");
}
__device__ __forceinline__ void ld1(v4u& r, const u8* base, u32 v0) {
  asm volatile("global_load_dwordx4 %0, %1, %2 sc1\n\ts_waitcnt vmcnt(0)" : "=&v"(r) : "v"(v0), "s"(base) : "memory");
}
#define CBAR() asm volatile("" ::: "memory")
#define SBAR() __builtin_amdgcn_sched_barrier(0)
#define PIN(x) asm volatile("" : "+v"(x))
constexpr int thr_of(int n) { return TA + (TB - TA) * n / 63; }

__device__ __forceinline__ void pace(u64 tz, int ticks) {
#pragma nounroll
  while ((int)(u32)(__builtin_amdgcn_s_memrealtime() - tz) < ticks) __builtin_amdgcn_s_sleep(2);
}
__device__ __forceinline__ void flag_set(LAS u8* L, int f, int w, int lane) {
  CBAR();
  if (lane == 0) *(volatile LAS u32*)(L + L_F + f * 16 + w * 4) = 1u;
  CBAR();
}
template <int SLEEP> __device__ __forceinline__ void flag_wait4(LAS u8* L, int f) {
#pragma nounroll
  for (int i = 0; i < SPIN_L; ++i) {
    v4u v = *(volatile LAS v4u*)(L + L_F + f * 16);
    if (__builtin_amdgcn_readfirstlane(v.x & v.y & v.z & v.w)) break;
    if (SLEEP) __builtin_amdgcn_s_sleep(SLEEP);
  }
  CBAR();
}
template <int SLEEP> __device__ __forceinline__ void flag_wait1(LAS u8* L, int f, int w) {
#pragma nounroll
  for (int i = 0; i < SPIN_L; ++i) {
    u32 v = *(volatile LAS u32*)(L + L_F + f * 16 + w * 4);
    if (__builtin_amdgcn_readfirstlane(v)) break;
    if (SLEEP) __builtin_amdgcn_s_sleep(SLEEP);
  }
  CBAR();
}
#define STAMP(i) do { } while (0)
#define STAMP_OUT() do { } while (0)
#define MKRSRC(p, n) __builtin_amdgcn_make_buffer_rsrc((void*)(p), 0, (int)(n), 0x00020000)

__global__ void __launch_bounds__(512, 2)
gqa_fused(const u8* x, const u8* kc, const u8* vc, const u8* wq, const u8* wo, u8* out, u8* ws) {
  extern __shared__ __attribute__((aligned(16))) u8 smem[];
  LAS u8* L = (LAS u8*)smem;
  const int tid = threadIdx.x, lane = tid & 63, wave = __builtin_amdgcn_readfirstlane(tid >> 6);
  const int g = lane >> 4, c = lane & 15;
  const int bid = blockIdx.x, kvh = bid & 7, j = bid >> 3, b = j >> 2, split = j & 3;
  const int wg = kvh * 32 + j;
  const int bh = j >> 4, hl = (j >> 2) & 3, nq = j & 3;
  STAMP(0);

  if (wave < 4) {
    const int w = wave;
    const u64 tz = __builtin_amdgcn_s_memrealtime();
    const int r8 = (w + 4 * (j & 1) + (j >> 1)) & 7;
    const size_t kvoff = ((size_t)((b * 8 + kvh) * 2048 + split * 512 + w * 128)) * 256;
    const u8* kb = kc + kvoff;
    const __amdgpu_buffer_rsrc_t rv = MKRSRC(vc + kvoff, 32768);
    u32 ko[4];
#pragma unroll
    for (int i = 0; i < 4; ++i) ko[i] = g * 256 + ((c ^ (4 * g + i)) * 16);
    const u32 ldsk = (u32)(size_t)L + L_K + w * 32768;
#pragma unroll
    for (int q4 = 0; q4 < 8; ++q4) {
#pragma unroll
      for (int i = 0; i < 4; ++i) {
        const int n = q4 * 4 + i;
        if (PEND && (n % PSTEP) == 0 && n >= PMIN) pace(tz, PEND - (63 - n) * PDELTA / 10);
        if (n >= thr_of(n)) asm volatile("s_waitcnt vmcnt(%0)" ::"i"(thr_of(q4 * 4 + i) - 1) : "memory");
        u32 keep;
        asm volatile("s_mov_b32 %0, m0\n\ts_mov_b32 m0, %3\n\ts_nop 0\n\tglobal_load_lds_dwordx4 %1, %2 offset:%4 nt\n\ts_mov_b32 m0, %0"
                     : "=&s"(keep) : "v"(ko[i]), "s"(kb + ((q4 + r8) & 7) * 4096), "s"(ldsk + q4 * 4096), "i"(i * 1024) : "memory");
        if (n == 0) STAMP(7);
        if (n == LFIRST) STAMP(8);
        if (n == TA) STAMP(9);
        if (n == 20) STAMP(10);
        if (n == 31) STAMP(11);
        if (n == QTRIG) flag_set(L, F_K2, w, lane);
        if (n == LFIRST - 1) {
          if (w == 0) {
            *(volatile LAS u32*)(L + L_F + lane * 4) = 0u;
            asm volatile("s_waitcnt lgkmcnt(0)" ::: "memory");
          }
          SBAR();
          __builtin_amdgcn_s_barrier();
          SBAR();
        }
      }
    }
    v4u v[32], vt[24];
#pragma unroll
    for (int i = 0; i < 32; ++i) {
      if (PEND && (i % PSTEP) == 0 && 32 + i >= PMIN) pace(tz, PEND - (31 - i) * PDELTA / 10);
      asm volatile("s_waitcnt vmcnt(%0)" ::"i"(thr_of(32 + i) - 1) : "memory");
      if (i >= thr_of(32 + i) - 1 && (i == 0 || i - 1 < thr_of(31 + i) - 1)) { flag_set(L, F_K, w, lane); STAMP(1); }
      v[i] = __builtin_amdgcn_raw_buffer_load_b128(rv, lane * 16 + (i & 3) * 1024, (((i >> 2) + r8) & 7) * 4096, 2);
    }
    SBAR();
    flag_set(L, F_V, w, lane);
    STAMP(2);
    const __amdgpu_buffer_rsrc_t rs = MKRSRC(ws, WS_END);
    flag_wait4<1>(L, F_P);
    __builtin_amdgcn_s_setprio(3);
    v4u p[4];
#pragma unroll
    for (int t = 0; t < 4; ++t) p[t] = *(LAS v4u*)(L + L_P + w * 4096 + t * 1024 + lane * 16);
    const u32 tag = __builtin_amdgcn_readfirstlane(*(LAS u32*)(L + L_T));
    v4u stv;
    {
      const f4 mm = *(LAS f4*)(L + L_M + (4 * w + (lane & 3)) * 16), ll = *(LAS f4*)(L + L_L + (4 * w + (lane & 3)) * 16);
      stv = v4u{bf(fmaxf(fmaxf(mm[0], mm[1]), fmaxf(mm[2], mm[3])) * SCL), tag, bf((ll[0] + ll[1]) + (ll[2] + ll[3])), tag};
    }
    const u32 obase = WS_BOX + (kvh * 32 + b * 4 + w) * BOX + 8192;
    u32 ooff = obase + split * 2048 + lane * 16;
    u32 soff = lane < HREP * 4 ? obase + 8192 + (lane >> 2) * 512 + split * 128 + (lane & 3) * 16 : 0x7FFFFFF0u;
    u32 rsrc_ = L_K + ((lane >> 2) & 3) * 2112 + ((lane >> 4) & 1) * 1024 + ((lane >> 5) * 16 + 4 * w + (lane & 3)) * 16;
    u32 wdst_ = L_K + w * 32768 + lane * 16;
    PIN(rsrc_); PIN(wdst_); PIN(ooff); PIN(soff); PIN(stv);
    STAMP(6);
    f4 o[8];
#pragma unroll
    for (int e = 0; e < 8; ++e) o[e] = f4{0.f, 0.f, 0.f, 0.f};
#pragma unroll
    for (int t = 0; t < 4; ++t) {
#pragma unroll
      for (int e = 0; e < 8; ++e) o[e] = mfma((EARLYT && t < 2) ? vt[8 * t + e] : tr8(&v[8 * t], e), p[t], o[e]);
      if (t < 3) SBAR();
    }
    STAMP(3);
#pragma unroll
    for (int e = 0; e < 8; ++e) *(LAS f4*)(L + wdst_ + (e >> 1) * 2112 + (e & 1) * 1024) = o[e];
    flag_set(L, F_O, w, lane);
    flag_wait4<0>(L, F_O);
    {
      LAS u8* src = L + rsrc_;
      f4 s0 = *(LAS f4*)src, s1 = *(LAS f4*)(src + 512);
#pragma unroll
      for (int ww = 1; ww < 4; ++ww) {
        s0 += *(LAS f4*)(src + ww * 32768);
        s1 += *(LAS f4*)(src + ww * 32768 + 512);
      }
      __builtin_amdgcn_raw_buffer_store_b128(v4u{pk(s0[0], s0[1]), tag, pk(s0[2], s0[3]), tag}, rs, ooff, 0, 16);
      __builtin_amdgcn_raw_buffer_store_b128(v4u{pk(s1[0], s1[1]), tag, pk(s1[2], s1[3]), tag}, rs, ooff, 1024, 16);
      SBAR();
      __builtin_amdgcn_raw_buffer_store_b128(stv, rs, soff, 0, 16);
    }
    STAMP(4);
    if (w == 0) {
      __builtin_amdgcn_s_setprio(0);
      const u32 base = WS_BOX + wg * BOX + lane * 16;
      const int u = kvh * 4 + hl, row16 = u >> 1, eh = u & 1;
      u32 oo = ((bh * 16 + row16) * 512 + nq * 128 + 8 * c + 4 * eh + g) * 2;
      PIN(oo);
      v4u r[8];
#pragma unroll
      for (int k = 0; k < 8; ++k) r[k] = v4u{0u, 0u, 0u, 0u};
      u32 todo = 255u;
      flag_wait4<1>(L, F_A);
      __builtin_amdgcn_s_setprio(3);
      if (FDELAY) __builtin_amdgcn_s_sleep(FDELAY);
      f4 s;
#pragma nounroll
      for (int it = 0; it < SPIN_G; ++it) {
        fetch8(r, (u32)__builtin_amdgcn_readfirstlane(todo), ws, base, base + 4096);
        u32 bk[8];
#pragma unroll
        for (int k = 0; k < 8; ++k) bk[k] = orxor(r[k].y ^ tag, r[k].w, tag);
        const u32 bad = ((bk[0] | bk[1]) | (bk[2] | bk[3] | bk[4])) | (bk[5] | bk[6] | bk[7]);
        f4 h[4];
#pragma unroll
        for (int k = 0; k < 4; ++k)
          h[k] = f4{fb(r[2 * k].x << 16), fb(r[2 * k].x & 0xFFFF0000u), fb(r[2 * k].z << 16), fb(r[2 * k].z & 0xFFFF0000u)} +
                 f4{fb(r[2 * k + 1].x << 16), fb(r[2 * k + 1].x & 0xFFFF0000u), fb(r[2 * k + 1].z << 16), fb(r[2 * k + 1].z & 0xFFFF0000u)};
        s = (h[0] + h[1]) + (h[2] + h[3]);
        PIN(s);
        if (!__builtin_amdgcn_ballot_w64(bad != 0u)) break;
        u32 nt = 0;
#pragma unroll
        for (int k = 0; k < 8; ++k) nt |= __builtin_amdgcn_ballot_w64(bk[k] != 0u) ? (1u << k) : 0u;
        todo = nt;
        if (FSLEEP) __builtin_amdgcn_s_sleep(FSLEEP);
      }
      const float tot = rsum4(s);
      const __bf16 ob = (__bf16)tot;
      *(GAS unsigned short*)(out + oo) = __builtin_bit_cast(unsigned short, ob);
      if ((bid & 127) == 0 && lane == 0) __builtin_amdgcn_raw_buffer_store_b32(tag, rs, WS_EPOCH + bh * 256, 0, 16);
      STAMP(5);
    }
    STAMP_OUT();
    return;
  }

  const int cw = wave - 4, ct = tid - 256;
  const __amdgpu_buffer_rsrc_t rs = MKRSRC(ws, WS_END);
  __builtin_amdgcn_s_setprio(3);
  const int qi = j & 7;
  const int qrot = (j >> 3) * 4;
  u32 tag;
  v4u wv[16];
  {
    v4u xa[2];
    const u32 ep = __builtin_amdgcn_raw_buffer_load_b32(rs, ct == 0 ? WS_EPOCH + bh * 256 : 0x7FFFFFF0u, 0, 16);
    const __amdgpu_buffer_rsrc_t rx = MKRSRC(x, 32768);
    const __amdgpu_buffer_rsrc_t rq = MKRSRC(wq, 4u << 20);
    const u32 xo = (((b & ~1) * 4 + (c & 7)) * 512 + cw * 128 + (2 * g + (c >> 3)) * 8) * 2;
#pragma unroll
    for (int T = 0; T < 2; ++T) xa[T] = __builtin_amdgcn_raw_buffer_load_b128(rx, xo, (T ^ (qrot >> 3)) * 128, 0);
    SBAR();
    const u32 wqo = ((cw * 128 + (lane >> 3) * 8) * 4096 + kvh * 512 + qi * 64 + (lane & 7) * 8) * 2;
#pragma unroll
    for (int i = 0; i < 16; ++i) {
      const int ii = i ^ qrot;
      wv[i] = __builtin_amdgcn_raw_buffer_load_b128(rq, wqo, ((ii >> 3) * 64 + (ii & 7)) * 8192, 0);
      if (i == CFIRST - 1) { SBAR(); __builtin_amdgcn_s_barrier(); SBAR(); }
    }
    SBAR();
    STAMP(11);
    if (qrot & 4) {
#pragma unroll
      for (int T = 0; T < 2; ++T) xa[T] = v4u{xa[T].z, xa[T].w, xa[T].x, xa[T].y};
    }
    f4 qa[8];
#pragma unroll
    for (int e = 0; e < 8; ++e) qa[e] = f4{0.f, 0.f, 0.f, 0.f};
#pragma unroll
    for (int T = 0; T < 2; ++T)
#pragma unroll
      for (int e = 0; e < 8; ++e) qa[e] = mfma(xa[T], tr8(&wv[8 * T], e), qa[e]);
    if ((g >> 1) == (c >> 3)) {
#pragma unroll
      for (int r = 0; r < 4; ++r) {
        LAS u8* dst = L + L_QP + cw * 4096 + (g * 4 + r) * 256 + (c & 7) * 32;
        *(LAS f4*)dst = f4{qa[0][r], qa[1][r], qa[2][r], qa[3][r]};
        *(LAS f4*)(dst + 16) = f4{qa[4][r], qa[5][r], qa[6][r], qa[7][r]};
      }
    }
    if (ct == 0) *(LAS u32*)(L + L_T) = ep + 1u;
    flag_set(L, F_QP, cw, lane);
    flag_wait4<0>(L, F_QP);
    tag = __builtin_amdgcn_readfirstlane(*(LAS u32*)(L + L_T));
    if (cw < 2) {
      const int row = ct >> 4;
      f4 s = {0.f, 0.f, 0.f, 0.f};
#pragma unroll
      for (int ww = 0; ww < 8; ++ww) s += *(LAS f4*)(L + L_QP + ww * 2048 + row * 256 + c * 16);
      const u32 off = WS_QBUF + ((((b & ~1) + (row >> 2)) * 8 + kvh) * 16 + (qi >> 1) * 4 + (row & 3)) * 512 + (qi & 1) * 256 + c * 16;
      __builtin_amdgcn_raw_buffer_store_b128(v4u{pk(s[0], s[1]), tag, pk(s[2], s[3]), tag}, rs, off, 0, 16);
    }
    SBAR();
    STAMP(2);
  }
  {
    const __amdgpu_buffer_rsrc_t ro = MKRSRC(wo + (size_t)(kvh * 4 + hl) * 131072, 131072);
    const u32 woo = (cw * 32 + 2 * g) * 1024 + nq * 256 + c * 16;
#pragma unroll
    for (int i = 0; i < 8; ++i) wv[i] = __builtin_amdgcn_raw_buffer_load_b128(ro, woo, ((i & 3) * 8 + ((i >> 2) ^ bh)) * 1024, 0);
    SBAR();
  }
  flag_wait1<4>(L, F_K2, cw);
  STAMP(1);
  {
    const u32 base = WS_QBUF + ((b * 8 + kvh) * 16 + cw * 4) * 512 + lane * 16;
    v4u r[2] = {v4u{0u, 0u, 0u, 0u}, v4u{0u, 0u, 0u, 0u}};
    u64 m0 = ~0ull, m1 = ~0ull;
#pragma nounroll
    for (int it = 0; it < SPIN_G; ++it) {
      poll2(r, m0, m1, ws, base);
      m0 = stale(r[0], tag);
      m1 = stale(r[1], tag);
      if (!(m0 | m1)) break;
    }
    LAS u8* dst = L + L_Q + (((lane >> 3) & 3) * 64 + ((lane >> 1) & 3) * 16 + cw * 4 + (lane >> 5)) * 16 + (lane & 1) * 8;
    *(LAS v2u*)dst = v2u{r[0].x, r[0].z};
    *(LAS v2u*)(dst + 32) = v2u{r[1].x, r[1].z};
    STAMP(3);
    flag_set(L, F_Q, cw, lane);
    flag_wait4<1>(L, F_Q);
  }
  v4u qf[4];
#pragma unroll
  for (int s = 0; s < 4; ++s) qf[s] = *(LAS v4u*)(L + L_Q + s * 1024 + lane * 16);
  flag_wait1<1>(L, F_K, cw);
  STAMP(4);
  f4 sc[8];
  {
    LAS u8* kr = L + L_K + cw * 32768 + (4 * (c & 3) + (c >> 2)) * 256;
    v4u kf[32];
#pragma unroll
    for (int u = 0; u < 8; ++u)
#pragma unroll
      for (int s = 0; s < 4; ++s) kf[u * 4 + s] = *(LAS v4u*)(kr + u * 4096 + ((4 * s + g) ^ c) * 16);
    SBAR();
#pragma unroll
    for (int u = 0; u < 8; ++u) {
      sc[u] = f4{0.f, 0.f, 0.f, 0.f};
#pragma unroll
      for (int s = 0; s < 4; ++s) sc[u] = mfma(kf[u * 4 + s], qf[s], sc[u]);
    }
  }
  {
    float m = sc[0][0];
#pragma unroll
    for (int u = 0; u < 8; ++u)
#pragma unroll
      for (int r = 0; r < 4; ++r) m = fmaxf(m, sc[u][r]);
    m = xmax(m);
    if (lane < 16) *(LAS float*)(L + L_M + c * 16 + cw * 4) = m;
    flag_set(L, F_M, cw, lane);
#pragma unroll
    for (int e = 0; e < 8; ++e) {
      const v4u fr = tr8(wv, e);
      LAS u8* d = L + L_K + cw * 32768 + 16384 + e * 1024 + lane * 16;
      *(LAS v4u*)d = bh ? v4u{fr.z, fr.x, fr.w, fr.y} : v4u{fr.x, fr.z, fr.y, fr.w};
    }
    flag_wait4<0>(L, F_M);
    const f4 mm = *(LAS f4*)(L + L_M + c * 16);
    const float M = fmaxf(fmaxf(mm[0], mm[1]), fmaxf(mm[2], mm[3])) * SCL;
    float l = 0.f;
#pragma unroll
    for (int u = 0; u < 8; ++u)
#pragma unroll
      for (int r = 0; r < 4; ++r) {
        sc[u][r] = __builtin_amdgcn_exp2f(__builtin_fmaf(sc[u][r], SCL, -M));
        l += sc[u][r];
      }
    l = xsum(l);
    if (lane < 16) *(LAS float*)(L + L_L + c * 16 + cw * 4) = l;
#pragma unroll
    for (int t = 0; t < 4; ++t) {
      const v4u pv = {pk(sc[2 * t][0], sc[2 * t][1]), pk(sc[2 * t][2], sc[2 * t][3]), pk(sc[2 * t + 1][0], sc[2 * t + 1][1]), pk(sc[2 * t + 1][2], sc[2 * t + 1][3])};
      *(LAS v4u*)(L + L_P + cw * 4096 + t * 1024 + lane * 16) = pv;
    }
    flag_set(L, F_P, cw, lane);
    STAMP(5);
  }
  __builtin_amdgcn_s_setprio(0);
  flag_wait4<1>(L, F_P);
  v4u wf[32];
#pragma unroll
  for (int t = 0; t < 4; ++t)
#pragma unroll
    for (int e = 0; e < 8; ++e) wf[t * 8 + e] = *(LAS v4u*)(L + L_K + t * 32768 + 16384 + e * 1024 + lane * 16);
#pragma unroll
  for (int i = 0; i < 32; ++i) PIN(wf[i]);
  STAMP(6);
  {
    const u32 obase = WS_BOX + (kvh * 32 + (bh * 4 + cw) * 4 + hl) * BOX + 8192;
    u32 v0 = obase + (c >> 2) * 2048 + g * 64 + (c & 3) * 16;
    const u32 vs = obase + 8192 + (HREP > 1 ? nq * 512 : 0) + g * 128 + (c & 3) * 16;
    const int u = (cw * 4 + g) * 2;
    u32 off = WS_BOX + ((u >> 2) * 32 + bh * 16 + (u & 3) * 4 + nq) * BOX + (kvh * 4 + hl) * 256 + c * 16;
    PIN(off); PIN(v0);
    flag_wait4<2>(L, CSTART);
    STAMP(8);
    v4u st;
#pragma nounroll
    for (int it = 0; it < SPIN_G; ++it) {
      ld1(st, ws, vs);
      if (!stale(st, tag)) break;
      if (CSLEEP) __builtin_amdgcn_s_sleep(CSLEEP);
    }
    __builtin_amdgcn_s_setprio(3);
    if (HDELAY) __builtin_amdgcn_s_sleep(HDELAY);
    STAMP(7);
    float res[8];
#pragma nounroll
    for (int it = 0; it < SPIN_G; ++it) {
      v4u r[8];
      ld8_issue(r, ws, v0);
      SBAR();
      const float mx = xmax(fb(st.x));
      const float ex = __builtin_amdgcn_exp2f(fb(st.x) - mx);
      const float fq = ex * __builtin_amdgcn_rcpf(xsum(ex * fb(st.z)));
      f4 f = {dppf<0x00>(fq), dppf<0x55>(fq), dppf<0xAA>(fq), dppf<0xFF>(fq)};
      PIN(f);
      ld8_wait(r);
      STAMP(9);
      u32 bad = 0;
#pragma unroll
      for (int t = 0; t < 4; ++t) {
        bad = orxor(orxor(bad, r[2 * t].y, tag), r[2 * t].w, tag);
        r[2 * t].y = r[2 * t + 1].x;
        r[2 * t].w = r[2 * t + 1].z;
      }
#pragma unroll
      for (int e = 0; e < 8; ++e) {
        f4 d = {0.f, 0.f, 0.f, 0.f};
#pragma unroll
        for (int t = 0; t < 4; ++t) d = mfma(r[2 * t], wf[t * 8 + e], d);
        if (e < 4) bad = orxor(orxor(bad, r[2 * e + 1].y, tag), r[2 * e + 1].w, tag);
        res[e] = rsum4(d * f);
      }
      if (!__builtin_amdgcn_ballot_w64(bad != 0u)) break;
    }
    __builtin_amdgcn_raw_buffer_store_b128(v4u{pk(res[0], res[1]), tag, pk(res[2], res[3]), tag}, rs, off, 0, 16);
    __builtin_amdgcn_raw_buffer_store_b128(v4u{pk(res[4], res[5]), tag, pk(res[6], res[7]), tag}, rs, off, 4 * BOX, 16);
    flag_set(L, F_A, cw, lane);
    STAMP(10);
  }
  STAMP_OUT();
}

extern "C" void kernel_launch(void* const* d_in, const int* in_sizes, int n_in, void* d_out, int out_size, void* d_ws,
                              size_t ws_size, hipStream_t stream) {
  (void)in_sizes; (void)n_in; (void)out_size; (void)ws_size;
  (void)hipFuncSetAttribute((const void*)gqa_fused, hipFuncAttributeMaxDynamicSharedMemorySize, (int)LDS_BYTES);
  hipLaunchKernelGGL(gqa_fused, dim3(256), dim3(512), LDS_BYTES, stream, (const u8*)d_in[0], (const u8*)d_in[1], (const u8*)d_in[2],
                     (const u8*)d_in[3], (const u8*)d_in[4], (u8*)d_out, (u8*)d_ws);
}
```

```cpp
#define TA 11
#define TB 11
#define CODEKB 13
#define EARLYT 0
#define FDELAY 12
#include <hip/hip_runtime.h>
#include <stdint.h>

typedef unsigned u32;
typedef unsigned long long u64;
typedef unsigned char u8;
typedef u32 v4u __attribute__((ext_vector_type(4)));
typedef u32 v2u __attribute__((ext_vector_type(2)));
typedef float f4 __attribute__((ext_vector_type(4)));
typedef float f2 __attribute__((ext_vector_type(2)));
typedef short s8v __attribute__((ext_vector_type(8)));
typedef __bf16 bf2v __attribute__((ext_vector_type(2)));
#define GAS __attribute__((address_space(1)))
#define LAS __attribute__((address_space(3)))

#define LFIRST 4
#define CFIRST 8
#define QTRIG 28
#define CSTART F_V
#define CSLEEP 1
#define HINTPOS 1
#define HDELAY 0
#define HREP 1
#define L0ALL 1
#define PEND 0
#define PDELTA 142
#define PSTEP 2
#define PMIN 16
#define CODEPF 1
#define CODEBACK 0
#define OUTAUX -1
#define FSLEEP 0
#define STAMPS 1

constexpr u32 WS_EPOCH = 0;
constexpr u32 WS_QBUF = 4096;
constexpr u32 WS_BOX = WS_QBUF + 64 * 8192;
constexpr u32 BOX = 18432;
constexpr u32 WS_END = WS_BOX + 256 * BOX;
constexpr u32 WS_STAMP = 8u << 20;
constexpr u32 L_K = 0;
constexpr u32 L_P = 131072;
constexpr u32 L_QP = L_P, L_Q = L_P + 16384;
constexpr u32 L_M = L_Q + 4096, L_L = L_M + 256, L_T = L_L + 256, L_F = L_T + 64;
constexpr u32 LDS_BYTES = L_F + 256;
enum { F_QP = 0, F_Q, F_K, F_M, F_P, F_O, F_V, F_A, F_K2 };

constexpr int SPIN_G = 1 << 17;
constexpr int SPIN_L = 1 << 22;
constexpr float SCL = 0.08838834764831845f * 1.4426950408889634f;

__device__ __forceinline__ f4 mfma(v4u a, v4u b, f4 c) {
  return __builtin_amdgcn_mfma_f32_16x16x32_bf16(__builtin_bit_cast(s8v, a), __builtin_bit_cast(s8v, b), c, 0, 0, 0);
}
__device__ __forceinline__ u32 pk(float lo, float hi) {
  f2 v = {lo, hi};
  return __builtin_bit_cast(u32, __builtin_convertvector(v, bf2v));
}
__device__ __forceinline__ u32 plo(u32 a, u32 b) { return __builtin_amdgcn_perm(b, a, 0x05040100u); }
__device__ __forceinline__ u32 phi(u32 a, u32 b) { return __builtin_amdgcn_perm(b, a, 0x07060302u); }
__device__ __forceinline__ v4u tr8(const v4u* v, int e) {
  v4u r;
#pragma unroll
  for (int jj = 0; jj < 4; ++jj) {
    const u32 a0 = v[2 * jj][e >> 1], a1 = v[2 * jj + 1][e >> 1];
    r[jj] = (e & 1) ? phi(a0, a1) : plo(a0, a1);
  }
  return r;
}
__device__ __forceinline__ float fb(u32 x) { return __builtin_bit_cast(float, x); }
__device__ __forceinline__ u32 bf(float x) { return __builtin_bit_cast(u32, x); }
__device__ __forceinline__ float xmax(float x) {
  auto s = __builtin_amdgcn_permlane16_swap(bf(x), bf(x), false, false);
  x = fmaxf(fb(s[0]), fb(s[1]));
  auto t = __builtin_amdgcn_permlane32_swap(bf(x), bf(x), false, false);
  return fmaxf(fb(t[0]), fb(t[1]));
}
__device__ __forceinline__ float xsum(float x) {
  auto s = __builtin_amdgcn_permlane16_swap(bf(x), bf(x), false, false);
  x = fb(s[0]) + fb(s[1]);
  auto t = __builtin_amdgcn_permlane32_swap(bf(x), bf(x), false, false);
  return fb(t[0]) + fb(t[1]);
}
__device__ __forceinline__ float rsum4(f4 d) {
  auto s02 = __builtin_amdgcn_permlane32_swap(bf(d[0]), bf(d[2]), false, false);
  auto s13 = __builtin_amdgcn_permlane32_swap(bf(d[1]), bf(d[3]), false, false);
  const float x02 = fb(s02[0]) + fb(s02[1]), x13 = fb(s13[0]) + fb(s13[1]);
  auto s = __builtin_amdgcn_permlane16_swap(bf(x02), bf(x13), false, false);
  return fb(s[0]) + fb(s[1]);
}
template <int CTRL> __device__ __forceinline__ float dppf(float x) {
  return __builtin_bit_cast(float, __builtin_amdgcn_mov_dpp(__builtin_bit_cast(int, x), CTRL, 0xf, 0xf, true));
}
__device__ __forceinline__ u64 stale(v4u r, u32 tag) { return __builtin_amdgcn_ballot_w64(((r.y ^ tag) | (r.w ^ tag)) != 0u); }

__device__ __forceinline__ void poll2(v4u (&r)[2], u64 m0, u64 m1, const u8* base, u32 voff) {
  u64 sv;
  asm volatile("s_mov_b64 %2, exec\n\t"
               "s_mov_b64 exec, %5\n\tglobal_load_dwordx4 %0, %3, %4 sc1\n\t"
               "s_mov_b64 exec, %6\n\tglobal_load_dwordx4 %1, %3, %4 offset:1024 sc1\n\t"
               "s_mov_b64 exec, %2\n\ts_waitcnt vmcnt(0)"
               : "+v"(r[0]), "+v"(r[1]), "=&s"(sv) : "v"(voff), "s"(base), "s"(m0), "s"(m1) : "memory");
}
__device__ __forceinline__ void ld8_issue(v4u (&r)[8], const u8* base, u32 v0) {
  asm volatile("global_load_dwordx4 %0, %8, %9 sc1\n\tglobal_load_dwordx4 %1, %8, %9 offset:256 sc1\n\t"
               "global_load_dwordx4 %2, %8, %9 offset:512 sc1\n\tglobal_load_dwordx4 %3, %8, %9 offset:768 sc1\n\t"
               "global_load_dwordx4 %4, %8, %9 offset:1024 sc1\n\tglobal_load_dwordx4 %5, %8, %9 offset:1280 sc1\n\t"
               "global_load_dwordx4 %6, %8, %9 offset:1536 sc1\n\tglobal_load_dwordx4 %7, %8, %9 offset:1792 sc1"
               : "=&v"(r[0]), "=&v"(r[1]), "=&v"(r[2]), "=&v"(r[3]), "=&v"(r[4]), "=&v"(r[5]), "=&v"(r[6]), "=&v"(r[7])
               : "v"(v0), "s"(base) : "memory");
}
__device__ __forceinline__ void ld8_wait(v4u (&r)[8]) {
  asm volatile("s_waitcnt vmcnt(0)"
               : "+v"(r[0]), "+v"(r[1]), "+v"(r[2]), "+v"(r[3]), "+v"(r[4]), "+v"(r[5]), "+v"(r[6]), "+v"(r[7]) :: "memory");
}
__device__ __forceinline__ u32 orxor(u32 acc, u32 a, u32 t) { return __builtin_amdgcn_bitop3_b32(acc, a, t, 0xF6); }
__device__ __forceinline__ void fetch8(v4u (&r)[8], u32 want, const u8* base, u32 v0, u32 v1) {
  asm volatile("s_bitcmp1_b32 %11, 0\n\ts_cbranch_scc0 1f\n\tglobal_load_dwordx4 %0, %8, %10 sc1\n"
               "1:\n\ts_bitcmp1_b32 %11, 1\n\ts_cbranch_scc0 2f\n\tglobal_load_dwordx4 %1, %8, %10 offset:1024 sc1\n"
               "2:\n\ts_bitcmp1_b32 %11, 2\n\ts_cbranch_scc0 3f\n\tglobal_load_dwordx4 %2, %8, %10 offset:2048 sc1\n"
               "3:\n\ts_bitcmp1_b32 %11, 3\n\ts_cbranch_scc0 4f\n\tglobal_load_dwordx4 %3, %8, %10 offset:3072 sc1\n"
               "4:\n\ts_bitcmp1_b32 %11, 4\n\ts_cbranch_scc0 5f\n\tglobal_load_dwordx4 %4, %9, %10 sc1\n"
               "5:\n\ts_bitcmp1_b32 %11, 5\n\ts_cbranch_scc0 6f\n\tglobal_load_dwordx4 %5, %9, %10 offset:1024 sc1\n"
               "6:\n\ts_bitcmp1_b32 %11, 6\n\ts_cbranch_scc0 7f\n\tglobal_load_dwordx4 %6, %9, %10 offset:2048 sc1\n"
               "7:\n\ts_bitcmp1_b32 %11, 7\n\ts_cbranch_scc0 8f\n\tglobal_load_dwordx4 %7, %9, %10 offset:3072 sc1\n"
               "8:\n\ts_waitcnt vmcnt(0)"
               : "+v"(r[0]), "+v"(r[1]), "+v"(r[2]), "+v"(r[3]), "+v"(r[4]), "+v"(r[5]), "+v"(r[6]), "+v"(r[7])
               : "v"(v0), "v"(v1), "s"(base), "s"(want) : "memory", "scc");
}
__device__ __forceinline__ void ld1(v4u& r, const u8* base, u32 v0) {
  asm volatile("global_load_dwordx4 %0, %1, %2 sc1\n\ts_waitcnt vmcnt(0)" : "=&v"(r) : "v"(v0), "s"(base) : "memory");
}
#define CBAR() asm volatile("" ::: "memory")
#define SBAR() __builtin_amdgcn_sched_barrier(0)
#define PIN(x) asm volatile("" : "+v"(x))
constexpr int thr_of(int n) { return TA + (TB - TA) * n / 63; }

__device__ __forceinline__ void pace(u64 tz, int ticks) {
#pragma nounroll
  while ((int)(u32)(__builtin_amdgcn_s_memrealtime() - tz) < ticks) __builtin_amdgcn_s_sleep(2);
}
__device__ __forceinline__ void flag_set(LAS u8* L, int f, int w, int lane) {
  CBAR();
  if (lane == 0) *(volatile LAS u32*)(L + L_F + f * 16 + w * 4) = 1u;
  CBAR();
}
template <int SLEEP> __device__ __forceinline__ void flag_wait4(LAS u8* L, int f) {
#pragma nounroll
  for (int i = 0; i < SPIN_L; ++i) {
    v4u v = *(volatile LAS v4u*)(L + L_F + f * 16);
    if (__builtin_amdgcn_readfirstlane(v.x & v.y & v.z & v.w)) break;
    if (SLEEP) __builtin_amdgcn_s_sleep(SLEEP);
  }
  CBAR();
}
template <int SLEEP> __device__ __forceinline__ void flag_wait1(LAS u8* L, int f, int w) {
#pragma nounroll
  for (int i = 0; i < SPIN_L; ++i) {
    u32 v = *(volatile LAS u32*)(L + L_F + f * 16 + w * 4);
    if (__builtin_amdgcn_readfirstlane(v)) break;
    if (SLEEP) __builtin_amdgcn_s_sleep(SLEEP);
  }
  CBAR();
}
#define STAMP(i) do { SBAR(); stamp[i] = __builtin_amdgcn_s_memrealtime(); SBAR(); } while (0)
#define STAMP_OUT() do { if (lane == 0) { for (int i_ = 0; i_ < 12; ++i_) ((GAS u64*)(ws + WS_STAMP))[(blockIdx.x * 8 + wave) * 16 + i_] = stamp[i_]; } } while (0)
#define MKRSRC(p, n) __builtin_amdgcn_make_buffer_rsrc((void*)(p), 0, (int)(n), 0x00020000)

__global__ void __launch_bounds__(512, 2)
gqa_fused(const u8* x, const u8* kc, const u8* vc, const u8* wq, const u8* wo, u8* out, u8* ws) {
  extern __shared__ __attribute__((aligned(16))) u8 smem[];
  LAS u8* L = (LAS u8*)smem;
  const int tid = threadIdx.x, lane = tid & 63, wave = __builtin_amdgcn_readfirstlane(tid >> 6);
  const int g = lane >> 4, c = lane & 15;
  const int bid = blockIdx.x, kvh = bid & 7, j = bid >> 3, b = j >> 2, split = j & 3;
  const int wg = kvh * 32 + j;
  const int bh = j >> 4, hl = (j >> 2) & 3, nq = j & 3;
  u64 stamp[12];
  for (int i = 0; i < 12; ++i) stamp[i] = 0;
  STAMP(0);

  if (wave < 4) {
    const int w = wave;
    const u64 tz = __builtin_amdgcn_s_memrealtime();
    const int r8 = (w + 4 * (j & 1) + (j >> 1)) & 7;
    const size_t kvoff = ((size_t)((b * 8 + kvh) * 2048 + split * 512 + w * 128)) * 256;
    const u8* kb = kc + kvoff;
    const __amdgpu_buffer_rsrc_t rv = MKRSRC(vc + kvoff, 32768);
    u32 ko[4];
#pragma unroll
    for (int i = 0; i < 4; ++i) ko[i] = g * 256 + ((c ^ (4 * g + i)) * 16);
    const u32 ldsk = (u32)(size_t)L + L_K + w * 32768;
#pragma unroll
    for (int q4 = 0; q4 < 8; ++q4) {
#pragma unroll
      for (int i = 0; i < 4; ++i) {
        const int n = q4 * 4 + i;
        if (PEND && (n % PSTEP) == 0 && n >= PMIN) pace(tz, PEND - (63 - n) * PDELTA / 10);
        if (n >= thr_of(n)) asm volatile("s_waitcnt vmcnt(%0)" ::"i"(thr_of(q4 * 4 + i) - 1) : "memory");
        u32 keep;
        asm volatile("s_mov_b32 %0, m0\n\ts_mov_b32 m0, %3\n\ts_nop 0\n\tglobal_load_lds_dwordx4 %1, %2 offset:%4 nt\n\ts_mov_b32 m0, %0"
                     : "=&s"(keep) : "v"(ko[i]), "s"(kb + ((q4 + r8) & 7) * 4096), "s"(ldsk + q4 * 4096), "i"(i * 1024) : "memory");
        if (n == 0) STAMP(7);
        if (n == LFIRST) STAMP(8);
        if (n == TA) STAMP(9);
        if (n == 20) STAMP(10);
        if (n == 31) STAMP(11);
        if (n == QTRIG) flag_set(L, F_K2, w, lane);
        if (n == LFIRST - 1) {
          if (w == 0) {
            *(volatile LAS u32*)(L + L_F + lane * 4) = 0u;
            asm volatile("s_waitcnt lgkmcnt(0)" ::: "memory");
          }
          SBAR();
          __builtin_amdgcn_s_barrier();
          SBAR();
        }
      }
    }
    v4u v[32], vt[24];
#pragma unroll
    for (int i = 0; i < 32; ++i) {
      if (PEND && (i % PSTEP) == 0 && 32 + i >= PMIN) pace(tz, PEND - (31 - i) * PDELTA / 10);
      asm volatile("s_waitcnt vmcnt(%0)" ::"i"(thr_of(32 + i) - 1) : "memory");
      if (i >= thr_of(32 + i) - 1 && (i == 0 || i - 1 < thr_of(31 + i) - 1)) { flag_set(L, F_K, w, lane); STAMP(1); }
      v[i] = __builtin_amdgcn_raw_buffer_load_b128(rv, lane * 16 + (i & 3) * 1024, (((i >> 2) + r8) & 7) * 4096, 2);
    }
    SBAR();
    flag_set(L, F_V, w, lane);
    STAMP(2);
    const __amdgpu_buffer_rsrc_t rs = MKRSRC(ws, WS_END);
    flag_wait4<1>(L, F_P);
    __builtin_amdgcn_s_setprio(3);
    v4u p[4];
#pragma unroll
    for (int t = 0; t < 4; ++t) p[t] = *(LAS v4u*)(L + L_P + w * 4096 + t * 1024 + lane * 16);
    const u32 tag = __builtin_amdgcn_readfirstlane(*(LAS u32*)(L + L_T));
    v4u stv;
    {
      const f4 mm = *(LAS f4*)(L + L_M + (4 * w + (lane & 3)) * 16), ll = *(LAS f4*)(L + L_L + (4 * w + (lane & 3)) * 16);
      stv = v4u{bf(fmaxf(fmaxf(mm[0], mm[1]), fmaxf(mm[2], mm[3])) * SCL), tag, bf((ll[0] + ll[1]) + (ll[2] + ll[3])), tag};
    }
    const u32 obase = WS_BOX + (kvh * 32 + b * 4 + w) * BOX + 8192;
    u32 ooff = obase + split * 2048 + lane * 16;
    u32 soff = lane < HREP * 4 ? obase + 8192 + (lane >> 2) * 512 + split * 128 + (lane & 3) * 16 : 0x7FFFFFF0u;
    u32 rsrc_ = L_K + ((lane >> 2) & 3) * 2112 + ((lane >> 4) & 1) * 1024 + ((lane >> 5) * 16 + 4 * w + (lane & 3)) * 16;
    u32 wdst_ = L_K + w * 32768 + lane * 16;
    PIN(rsrc_); PIN(wdst_); PIN(ooff); PIN(soff); PIN(stv);
    STAMP(6);
    f4 o[8];
#pragma unroll
    for (int e = 0; e < 8; ++e) o[e] = f4{0.f, 0.f, 0.f, 0.f};
#pragma unroll
    for (int t = 0; t < 4; ++t) {
#pragma unroll
      for (int e = 0; e < 8; ++e) o[e] = mfma((EARLYT && t < 2) ? vt[8 * t + e] : tr8(&v[8 * t], e), p[t], o[e]);
      if (t < 3) SBAR();
    }
    STAMP(3);
#pragma unroll
    for (int e = 0; e < 8; ++e) *(LAS f4*)(L + wdst_ + (e >> 1) * 2112 + (e & 1) * 1024) = o[e];
    flag_set(L, F_O, w, lane);
    flag_wait4<0>(L, F_O);
    {
      LAS u8* src = L + rsrc_;
      f4 s0 = *(LAS f4*)src, s1 = *(LAS f4*)(src + 512);
#pragma unroll
      for (int ww = 1; ww < 4; ++ww) {
        s0 += *(LAS f4*)(src + ww * 32768);
        s1 += *(LAS f4*)(src + ww * 32768 + 512);
      }
      __builtin_amdgcn_raw_buffer_store_b128(v4u{pk(s0[0], s0[1]), tag, pk(s0[2], s0[3]), tag}, rs, ooff, 0, 16);
      __builtin_amdgcn_raw_buffer_store_b128(v4u{pk(s1[0], s1[1]), tag, pk(s1[2], s1[3]), tag}, rs, ooff, 1024, 16);
      SBAR();
      __builtin_amdgcn_raw_buffer_store_b128(stv, rs, soff, 0, 16);
    }
    STAMP(4);
    if (w == 0) {
      __builtin_amdgcn_s_setprio(0);
      const u32 base = WS_BOX + wg * BOX + lane * 16;
      const int u = kvh * 4 + hl, row16 = u >> 1, eh = u & 1;
      u32 oo = ((bh * 16 + row16) * 512 + nq * 128 + 8 * c + 4 * eh + g) * 2;
      PIN(oo);
      v4u r[8];
#pragma unroll
      for (int k = 0; k < 8; ++k) r[k] = v4u{0u, 0u, 0u, 0u};
      u32 todo = 255u;
      flag_wait4<1>(L, F_A);
      __builtin_amdgcn_s_setprio(3);
      if (FDELAY) __builtin_amdgcn_s_sleep(FDELAY);
      f4 s;
#pragma nounroll
      for (int it = 0; it < SPIN_G; ++it) {
        fetch8(r, (u32)__builtin_amdgcn_readfirstlane(todo), ws, base, base + 4096);
        u32 bk[8];
#pragma unroll
        for (int k = 0; k < 8; ++k) bk[k] = orxor(r[k].y ^ tag, r[k].w, tag);
        const u32 bad = ((bk[0] | bk[1]) | (bk[2] | bk[3] | bk[4])) | (bk[5] | bk[6] | bk[7]);
        f4 h[4];
#pragma unroll
        for (int k = 0; k < 4; ++k)
          h[k] = f4{fb(r[2 * k].x << 16), fb(r[2 * k].x & 0xFFFF0000u), fb(r[2 * k].z << 16), fb(r[2 * k].z & 0xFFFF0000u)} +
                 f4{fb(r[2 * k + 1].x << 16), fb(r[2 * k + 1].x & 0xFFFF0000u), fb(r[2 * k + 1].z << 16), fb(r[2 * k + 1].z & 0xFFFF0000u)};
        s = (h[0] + h[1]) + (h[2] + h[3]);
        PIN(s);
        if (!__builtin_amdgcn_ballot_w64(bad != 0u)) break;
        u32 nt = 0;
#pragma unroll
        for (int k = 0; k < 8; ++k) nt |= __builtin_amdgcn_ballot_w64(bk[k] != 0u) ? (1u << k) : 0u;
        todo = nt;
        if (FSLEEP) __builtin_amdgcn_s_sleep(FSLEEP);
      }
      const float tot = rsum4(s);
      const __bf16 ob = (__bf16)tot;
      *(GAS unsigned short*)(out + oo) = __builtin_bit_cast(unsigned short, ob);
      if ((bid & 127) == 0 && lane == 0) __builtin_amdgcn_raw_buffer_store_b32(tag, rs, WS_EPOCH + bh * 256, 0, 16);
      STAMP(5);
    }
    STAMP_OUT();
    return;
  }

  const int cw = wave - 4, ct = tid - 256;
  {
    u64 pc;
    asm volatile("s_getpc_b64 %0" : "=s"(pc));
    const u8* cb = (const u8*)(pc & ~255ull) - CODEBACK;
    if (cw == 3) {
      u32 keep;
      asm volatile("s_mov_b32 %0, m0\n\ts_mov_b32 m0, %3\n\ts_nop 0\n\tglobal_load_lds_dwordx4 %1, %2\n\ts_mov_b32 m0, %0"
                   : "=&s"(keep) : "v"((u32)((j % CODEKB) * 1024 + lane * 16)), "s"(cb), "s"((u32)(size_t)L + L_Q) : "memory");
    }
  }
  const __amdgpu_buffer_rsrc_t rs = MKRSRC(ws, WS_END);
  __builtin_amdgcn_s_setprio(3);
  const int qi = j & 7;
  const int qrot = (j >> 3) * 4;
  u32 tag;
  v4u wv[16];
  {
    v4u xa[2];
    const u32 ep = __builtin_amdgcn_raw_buffer_load_b32(rs, ct == 0 ? WS_EPOCH + bh * 256 : 0x7FFFFFF0u, 0, 16);
    const __amdgpu_buffer_rsrc_t rx = MKRSRC(x, 32768);
    const __amdgpu_buffer_rsrc_t rq = MKRSRC(wq, 4u << 20);
    const u32 xo = (((b & ~1) * 4 + (c & 7)) * 512 + cw * 128 + (2 * g + (c >> 3)) * 8) * 2;
#pragma unroll
    for (int T = 0; T < 2; ++T) xa[T] = __builtin_amdgcn_raw_buffer_load_b128(rx, xo, (T ^ (qrot >> 3)) * 128, 0);
    SBAR();
    const u32 wqo = ((cw * 128 + (lane >> 3) * 8) * 4096 + kvh * 512 + qi * 64 + (lane & 7) * 8) * 2;
#pragma unroll
    for (int i = 0; i < 16; ++i) {
      const int ii = i ^ qrot;
      wv[i] = __builtin_amdgcn_raw_buffer_load_b128(rq, wqo, ((ii >> 3) * 64 + (ii & 7)) * 8192, 0);
      if (i == CFIRST - 1) { SBAR(); __builtin_amdgcn_s_barrier(); SBAR(); }
    }
    SBAR();
    STAMP(11);
    if (qrot & 4) {
#pragma unroll
      for (int T = 0; T < 2; ++T) xa[T] = v4u{xa[T].z, xa[T].w, xa[T].x, xa[T].y};
    }
    f4 qa[8];
#pragma unroll
    for (int e = 0; e < 8; ++e) qa[e] = f4{0.f, 0.f, 0.f, 0.f};
#pragma unroll
    for (int T = 0; T < 2; ++T)
#pragma unroll
      for (int e = 0; e < 8; ++e) qa[e] = mfma(xa[T], tr8(&wv[8 * T], e), qa[e]);
    if ((g >> 1) == (c >> 3)) {
#pragma unroll
      for (int r = 0; r < 4; ++r) {
        LAS u8* dst = L + L_QP + cw * 4096 + (g * 4 + r) * 256 + (c & 7) * 32;
        *(LAS f4*)dst = f4{qa[0][r], qa[1][r], qa[2][r], qa[3][r]};
        *(LAS f4*)(dst + 16) = f4{qa[4][r], qa[5][r], qa[6][r], qa[7][r]};
      }
    }
    if (ct == 0) *(LAS u32*)(L + L_T) = ep + 1u;
    flag_set(L, F_QP, cw, lane);
    flag_wait4<0>(L, F_QP);
    tag = __builtin_amdgcn_readfirstlane(*(LAS u32*)(L + L_T));
    if (cw < 2) {
      const int row = ct >> 4;
      f4 s = {0.f, 0.f, 0.f, 0.f};
#pragma unroll
      for (int ww = 0; ww < 8; ++ww) s += *(LAS f4*)(L + L_QP + ww * 2048 + row * 256 + c * 16);
      const u32 off = WS_QBUF + ((((b & ~1) + (row >> 2)) * 8 + kvh) * 16 + (qi >> 1) * 4 + (row & 3)) * 512 + (qi & 1) * 256 + c * 16;
      __builtin_amdgcn_raw_buffer_store_b128(v4u{pk(s[0], s[1]), tag, pk(s[2], s[3]), tag}, rs, off, 0, 16);
    }
    SBAR();
    STAMP(2);
  }
  {
    const __amdgpu_buffer_rsrc_t ro = MKRSRC(wo + (size_t)(kvh * 4 + hl) * 131072, 131072);
    const u32 woo = (cw * 32 + 2 * g) * 1024 + nq * 256 + c * 16;
#pragma unroll
    for (int i = 0; i < 8; ++i) wv[i] = __builtin_amdgcn_raw_buffer_load_b128(ro, woo, ((i & 3) * 8 + ((i >> 2) ^ bh)) * 1024, 0);
    SBAR();
  }
  flag_wait1<4>(L, F_K2, cw);
  STAMP(1);
  {
    const u32 base = WS_QBUF + ((b * 8 + kvh) * 16 + cw * 4) * 512 + lane * 16;
    v4u r[2] = {v4u{0u, 0u, 0u, 0u}, v4u{0u, 0u, 0u, 0u}};
    u64 m0 = ~0ull, m1 = ~0ull;
#pragma nounroll
    for (int it = 0; it < SPIN_G; ++it) {
      poll2(r, m0, m1, ws, base);
      m0 = stale(r[0], tag);
      m1 = stale(r[1], tag);
      if (!(m0 | m1)) break;
    }
    LAS u8* dst = L + L_Q + (((lane >> 3) & 3) * 64 + ((lane >> 1) & 3) * 16 + cw * 4 + (lane >> 5)) * 16 + (lane & 1) * 8;
    *(LAS v2u*)dst = v2u{r[0].x, r[0].z};
    *(LAS v2u*)(dst + 32) = v2u{r[1].x, r[1].z};
    STAMP(3);
    flag_set(L, F_Q, cw, lane);
    flag_wait4<1>(L, F_Q);
  }
  v4u qf[4];
#pragma unroll
  for (int s = 0; s < 4; ++s) qf[s] = *(LAS v4u*)(L + L_Q + s * 1024 + lane * 16);
  flag_wait1<1>(L, F_K, cw);
  STAMP(4);
  f4 sc[8];
  {
    LAS u8* kr = L + L_K + cw * 32768 + (4 * (c & 3) + (c >> 2)) * 256;
    v4u kf[32];
#pragma unroll
    for (int u = 0; u < 8; ++u)
#pragma unroll
      for (int s = 0; s < 4; ++s) kf[u * 4 + s] = *(LAS v4u*)(kr + u * 4096 + ((4 * s + g) ^ c) * 16);
    SBAR();
#pragma unroll
    for (int u = 0; u < 8; ++u) {
      sc[u] = f4{0.f, 0.f, 0.f, 0.f};
#pragma unroll
      for (int s = 0; s < 4; ++s) sc[u] = mfma(kf[u * 4 + s], qf[s], sc[u]);
    }
  }
  {
    float m = sc[0][0];
#pragma unroll
    for (int u = 0; u < 8; ++u)
#pragma unroll
      for (int r = 0; r < 4; ++r) m = fmaxf(m, sc[u][r]);
    m = xmax(m);
    if (lane < 16) *(LAS float*)(L + L_M + c * 16 + cw * 4) = m;
    flag_set(L, F_M, cw, lane);
#pragma unroll
    for (int e = 0; e < 8; ++e) {
      const v4u fr = tr8(wv, e);
      LAS u8* d = L + L_K + cw * 32768 + 16384 + e * 1024 + lane * 16;
      *(LAS v4u*)d = bh ? v4u{fr.z, fr.x, fr.w, fr.y} : v4u{fr.x, fr.z, fr.y, fr.w};
    }
    flag_wait4<0>(L, F_M);
    const f4 mm = *(LAS f4*)(L + L_M + c * 16);
    const float M = fmaxf(fmaxf(mm[0], mm[1]), fmaxf(mm[2], mm[3])) * SCL;
    float l = 0.f;
#pragma unroll
    for (int u = 0; u < 8; ++u)
#pragma unroll
      for (int r = 0; r < 4; ++r) {
        sc[u][r] = __builtin_amdgcn_exp2f(__builtin_fmaf(sc[u][r], SCL, -M));
        l += sc[u][r];
      }
    l = xsum(l);
    if (lane < 16) *(LAS float*)(L + L_L + c * 16 + cw * 4) = l;
#pragma unroll
    for (int t = 0; t < 4; ++t) {
      const v4u pv = {pk(sc[2 * t][0], sc[2 * t][1]), pk(sc[2 * t][2], sc[2 * t][3]), pk(sc[2 * t + 1][0], sc[2 * t + 1][1]), pk(sc[2 * t + 1][2], sc[2 * t + 1][3])};
      *(LAS v4u*)(L + L_P + cw * 4096 + t * 1024 + lane * 16) = pv;
    }
    flag_set(L, F_P, cw, lane);
    STAMP(5);
  }
  __builtin_amdgcn_s_setprio(0);
  flag_wait4<1>(L, F_P);
  v4u wf[32];
#pragma unroll
  for (int t = 0; t < 4; ++t)
#pragma unroll
    for (int e = 0; e < 8; ++e) wf[t * 8 + e] = *(LAS v4u*)(L + L_K + t * 32768 + 16384 + e * 1024 + lane * 16);
#pragma unroll
  for (int i = 0; i < 32; ++i) PIN(wf[i]);
  STAMP(6);
  {
    const u32 obase = WS_BOX + (kvh * 32 + (bh * 4 + cw) * 4 + hl) * BOX + 8192;
    u32 v0 = obase + (c >> 2) * 2048 + g * 64 + (c & 3) * 16;
    const u32 vs = obase + 8192 + (HREP > 1 ? nq * 512 : 0) + g * 128 + (c & 3) * 16;
    const int u = (cw * 4 + g) * 2;
    u32 off = WS_BOX + ((u >> 2) * 32 + bh * 16 + (u & 3) * 4 + nq) * BOX + (kvh * 4 + hl) * 256 + c * 16;
    PIN(off); PIN(v0);
    flag_wait4<2>(L, CSTART);
    STAMP(8);
    v4u st;
#pragma nounroll
    for (int it = 0; it < SPIN_G; ++it) {
      ld1(st, ws, vs);
      if (!stale(st, tag)) break;
      if (CSLEEP) __builtin_amdgcn_s_sleep(CSLEEP);
    }
    __builtin_amdgcn_s_setprio(3);
    if (HDELAY) __builtin_amdgcn_s_sleep(HDELAY);
    STAMP(7);
    float res[8];
#pragma nounroll
    for (int it = 0; it < SPIN_G; ++it) {
      v4u r[8];
      ld8_issue(r, ws, v0);
      SBAR();
      const float mx = xmax(fb(st.x));
      const float ex = __builtin_amdgcn_exp2f(fb(st.x) - mx);
      const float fq = ex * __builtin_amdgcn_rcpf(xsum(ex * fb(st.z)));
      f4 f = {dppf<0x00>(fq), dppf<0x55>(fq), dppf<0xAA>(fq), dppf<0xFF>(fq)};
      PIN(f);
      ld8_wait(r);
      STAMP(9);
      u32 bad = 0;
#pragma unroll
      for (int t = 0; t < 4; ++t) {
        bad = orxor(orxor(bad, r[2 * t].y, tag), r[2 * t].w, tag);
        r[2 * t].y = r[2 * t + 1].x;
        r[2 * t].w = r[2 * t + 1].z;
      }
#pragma unroll
      for (int e = 0; e < 8; ++e) {
        f4 d = {0.f, 0.f, 0.f, 0.f};
#pragma unroll
        for (int t = 0; t < 4; ++t) d = mfma(r[2 * t], wf[t * 8 + e], d);
        if (e < 4) bad = orxor(orxor(bad, r[2 * e + 1].y, tag), r[2 * e + 1].w, tag);
        res[e] = rsum4(d * f);
      }
      if (!__builtin_amdgcn_ballot_w64(bad != 0u)) break;
    }
    __builtin_amdgcn_raw_buffer_store_b128(v4u{pk(res[0], res[1]), tag, pk(res[2], res[3]), tag}, rs, off, 0, 16);
    __builtin_amdgcn_raw_buffer_store_b128(v4u{pk(res[4], res[5]), tag, pk(res[6], res[7]), tag}, rs, off, 4 * BOX, 16);
    flag_set(L, F_A, cw, lane);
    STAMP(10);
  }
  STAMP_OUT();
}

__global__ void probe(const u8* ws) {
  __shared__ u64 smn[256], smx[256], ssum[256]; __shared__ int scnt[256];
  const u64* st = (const u64*)(ws + WS_STAMP);
  const int t = threadIdx.x;
  u64 mn = ~0ull;
  for (int w = 0; w < 8; ++w) { u64 v = st[(t * 8 + w) * 16]; if (v < mn) mn = v; }
  smn[t] = mn; __syncthreads();
  if (t == 0) { for (int i = 1; i < 256; ++i) if (smn[i] < smn[0]) smn[0] = smn[i]; }
  __syncthreads(); mn = smn[0]; __syncthreads();
  u64 lo = ~0ull, hi = 0, sum = 0; int n = 0;
  if (0) { for (int w = 0; w < 8; ++w) for (int i = 0; i < 12; ++i) { u64 v = st[(t * 8 + w) * 16 + i]; if (v && v - mn > hi) hi = v - mn; } }
  else for (int w = 0 * 4; w < 0 * 4 + 4; ++w) { u64 v = st[(t * 8 + w) * 16 + 0]; if (!v) continue; u64 d = v - mn; sum += d; n++; if (d < lo) lo = d; if (d > hi) hi = d; }
  smn[t] = lo; smx[t] = hi; ssum[t] = sum; scnt[t] = n; __syncthreads();
  if (t == 0) {
    for (int i = 1; i < 256; ++i) { if (smn[i] < smn[0]) smn[0] = smn[i]; if (smx[i] > smx[0]) smx[0] = smx[i]; ssum[0] += ssum[i]; scnt[0] += scnt[i]; }
    u64 val = smn[0];
    if (val > 20000) val = 20000;
    const u64 t0 = __builtin_amdgcn_s_memrealtime();
    while (__builtin_amdgcn_s_memrealtime() - t0 < val * 100) __builtin_amdgcn_s_sleep(8);
  }
}
extern "C" void kernel_launch(void* const* d_in, const int* in_sizes, int n_in, void* d_out, int out_size, void* d_ws,
                              size_t ws_size, hipStream_t stream) {
  (void)in_sizes; (void)n_in; (void)out_size; (void)ws_size;
  (void)hipFuncSetAttribute((const void*)gqa_fused, hipFuncAttributeMaxDynamicSharedMemorySize, (int)LDS_BYTES);
  hipLaunchKernelGGL(gqa_fused, dim3(256), dim3(512), LDS_BYTES, stream, (const u8*)d_in[0], (const u8*)d_in[1], (const u8*)d_in[2],
                     (const u8*)d_in[3], (const u8*)d_in[4], (u8*)d_out, (u8*)d_ws);
  probe<<<1, 256, 0, stream>>>((const u8*)d_ws);
}
```

```cpp
#include <hip/hip_runtime.h>
#include <stdint.h>

typedef unsigned u32;
typedef unsigned long long u64;
typedef unsigned char u8;
typedef u32 v4u __attribute__((ext_vector_type(4)));
typedef u32 v2u __attribute__((ext_vector_type(2)));
typedef float f4 __attribute__((ext_vector_type(4)));
typedef float f2 __attribute__((ext_vector_type(2)));
typedef short s8v __attribute__((ext_vector_type(8)));
typedef __bf16 bf2v __attribute__((ext_vector_type(2)));
#define GAS __attribute__((address_space(1)))
#define LAS __attribute__((address_space(3)))

#define THR 12
#define BURST 4
#define LFIRST 4
#define CFIRST 8
#define QTRIG 28
#define FDELAY 12
#define CODEPF 1
#define STAMPS 0

constexpr u32 WS_EPOCH = 0;
constexpr u32 WS_QBUF = 4096;
constexpr u32 WS_BOX = WS_QBUF + 64 * 8192;
constexpr u32 BOX = 16896;
constexpr u32 WS_END = WS_BOX + 256 * BOX;
constexpr u32 WS_STAMP = 8u << 20;
constexpr u32 L_K = 0;
constexpr u32 L_P = 131072;
constexpr u32 L_QP = L_P, L_Q = L_P + 16384;
constexpr u32 L_M = L_Q + 4096, L_L = L_M + 256, L_T = L_L + 256, L_F = L_T + 64;
constexpr u32 LDS_BYTES = L_F + 256;
enum { F_QP = 0, F_Q, F_K, F_M, F_P, F_O, F_V, F_A, F_K2 };

constexpr int SPIN_G = 1 << 17;
constexpr int SPIN_L = 1 << 22;
constexpr float SCL = 0.08838834764831845f * 1.4426950408889634f;

__device__ __forceinline__ f4 mfma(v4u a, v4u b, f4 c) {
  return __builtin_amdgcn_mfma_f32_16x16x32_bf16(__builtin_bit_cast(s8v, a), __builtin_bit_cast(s8v, b), c, 0, 0, 0);
}
__device__ __forceinline__ u32 pk(float lo, float hi) {
  f2 v = {lo, hi};
  return __builtin_bit_cast(u32, __builtin_convertvector(v, bf2v));
}
__device__ __forceinline__ u32 plo(u32 a, u32 b) { return __builtin_amdgcn_perm(b, a, 0x05040100u); }
__device__ __forceinline__ u32 phi(u32 a, u32 b) { return __builtin_amdgcn_perm(b, a, 0x07060302u); }
__device__ __forceinline__ v4u tr8(const v4u* v, int e) {
  v4u r;
#pragma unroll
  for (int jj = 0; jj < 4; ++jj) {
    const u32 a0 = v[2 * jj][e >> 1], a1 = v[2 * jj + 1][e >> 1];
    r[jj] = (e & 1) ? phi(a0, a1) : plo(a0, a1);
  }
  return r;
}
__device__ __forceinline__ float fb(u32 x) { return __builtin_bit_cast(float, x); }
__device__ __forceinline__ u32 bf(float x) { return __builtin_bit_cast(u32, x); }
__device__ __forceinline__ float xmax(float x) {
  auto s = __builtin_amdgcn_permlane16_swap(bf(x), bf(x), false, false);
  x = fmaxf(fb(s[0]), fb(s[1]));
  auto t = __builtin_amdgcn_permlane32_swap(bf(x), bf(x), false, false);
  return fmaxf(fb(t[0]), fb(t[1]));
}
__device__ __forceinline__ float xsum(float x) {
  auto s = __builtin_amdgcn_permlane16_swap(bf(x), bf(x), false, false);
  x = fb(s[0]) + fb(s[1]);
  auto t = __builtin_amdgcn_permlane32_swap(bf(x), bf(x), false, false);
  return fb(t[0]) + fb(t[1]);
}
__device__ __forceinline__ float rsum4(f4 d) {
  auto s02 = __builtin_amdgcn_permlane32_swap(bf(d[0]), bf(d[2]), false, false);
  auto s13 = __builtin_amdgcn_permlane32_swap(bf(d[1]), bf(d[3]), false, false);
  const float x02 = fb(s02[0]) + fb(s02[1]), x13 = fb(s13[0]) + fb(s13[1]);
  auto s = __builtin_amdgcn_permlane16_swap(bf(x02), bf(x13), false, false);
  return fb(s[0]) + fb(s[1]);
}
template <int CTRL> __device__ __forceinline__ float dppf(float x) {
  return __builtin_bit_cast(float, __builtin_amdgcn_mov_dpp(__builtin_bit_cast(int, x), CTRL, 0xf, 0xf, true));
}
__device__ __forceinline__ u64 stale(v4u r, u32 tag) { return __builtin_amdgcn_ballot_w64(((r.y ^ tag) | (r.w ^ tag)) != 0u); }
__device__ __forceinline__ u32 orxor(u32 acc, u32 a, u32 t) { return __builtin_amdgcn_bitop3_b32(acc, a, t, 0xF6); }

__device__ __forceinline__ void poll2(v4u (&r)[2], u64 m0, u64 m1, const u8* base, u32 voff) {
  u64 sv;
  asm volatile("s_mov_b64 %2, exec\n\t"
               "s_mov_b64 exec, %5\n\tglobal_load_dwordx4 %0, %3, %4 sc1\n\t"
               "s_mov_b64 exec, %6\n\tglobal_load_dwordx4 %1, %3, %4 offset:1024 sc1\n\t"
               "s_mov_b64 exec, %2\n\ts_waitcnt vmcnt(0)"
               : "+v"(r[0]), "+v"(r[1]), "=&s"(sv) : "v"(voff), "s"(base), "s"(m0), "s"(m1) : "memory");
}
__device__ __forceinline__ void ld8_issue(v4u (&r)[8], const u8* base, u32 v0) {
  asm volatile("global_load_dwordx4 %0, %8, %9 sc1\n\tglobal_load_dwordx4 %1, %8, %9 offset:256 sc1\n\t"
               "global_load_dwordx4 %2, %8, %9 offset:512 sc1\n\tglobal_load_dwordx4 %3, %8, %9 offset:768 sc1\n\t"
               "global_load_dwordx4 %4, %8, %9 offset:1024 sc1\n\tglobal_load_dwordx4 %5, %8, %9 offset:1280 sc1\n\t"
               "global_load_dwordx4 %6, %8, %9 offset:1536 sc1\n\tglobal_load_dwordx4 %7, %8, %9 offset:1792 sc1"
               : "=&v"(r[0]), "=&v"(r[1]), "=&v"(r[2]), "=&v"(r[3]), "=&v"(r[4]), "=&v"(r[5]), "=&v"(r[6]), "=&v"(r[7])
               : "v"(v0), "s"(base) : "memory");
}
__device__ __forceinline__ void ld8_wait(v4u (&r)[8]) {
  asm volatile("s_waitcnt vmcnt(0)"
               : "+v"(r[0]), "+v"(r[1]), "+v"(r[2]), "+v"(r[3]), "+v"(r[4]), "+v"(r[5]), "+v"(r[6]), "+v"(r[7]) :: "memory");
}
__device__ __forceinline__ void fetch8(v4u (&r)[8], u32 want, const u8* base, u32 v0, u32 v1) {
  asm volatile("s_bitcmp1_b32 %11, 0\n\ts_cbranch_scc0 1f\n\tglobal_load_dwordx4 %0, %8, %10 sc1\n"
               "1:\n\ts_bitcmp1_b32 %11, 1\n\ts_cbranch_scc0 2f\n\tglobal_load_dwordx4 %1, %8, %10 offset:1024 sc1\n"
               "2:\n\ts_bitcmp1_b32 %11, 2\n\ts_cbranch_scc0 3f\n\tglobal_load_dwordx4 %2, %8, %10 offset:2048 sc1\n"
               "3:\n\ts_bitcmp1_b32 %11, 3\n\ts_cbranch_scc0 4f\n\tglobal_load_dwordx4 %3, %8, %10 offset:3072 sc1\n"
               "4:\n\ts_bitcmp1_b32 %11, 4\n\ts_cbranch_scc0 5f\n\tglobal_load_dwordx4 %4, %9, %10 sc1\n"
               "5:\n\ts_bitcmp1_b32 %11, 5\n\ts_cbranch_scc0 6f\n\tglobal_load_dwordx4 %5, %9, %10 offset:1024 sc1\n"
               "6:\n\ts_bitcmp1_b32 %11, 6\n\ts_cbranch_scc0 7f\n\tglobal_load_dwordx4 %6, %9, %10 offset:2048 sc1\n"
               "7:\n\ts_bitcmp1_b32 %11, 7\n\ts_cbranch_scc0 8f\n\tglobal_load_dwordx4 %7, %9, %10 offset:3072 sc1\n"
               "8:\n\ts_waitcnt vmcnt(0)"
               : "+v"(r[0]), "+v"(r[1]), "+v"(r[2]), "+v"(r[3]), "+v"(r[4]), "+v"(r[5]), "+v"(r[6]), "+v"(r[7])
               : "v"(v0), "v"(v1), "s"(base), "s"(want) : "memory", "scc");
}
__device__ __forceinline__ void ld1(v4u& r, const u8* base, u32 v0) {
  asm volatile("global_load_dwordx4 %0, %1, %2 sc1\n\ts_waitcnt vmcnt(0)" : "=&v"(r) : "v"(v0), "s"(base) : "memory");
}
#define CBAR() asm volatile("" ::: "memory")
#define SBAR() __builtin_amdgcn_sched_barrier(0)
#define PIN(x) asm volatile("" : "+v"(x))
#define WAITVM(n) asm volatile("s_waitcnt vmcnt(%0)" ::"i"(n) : "memory")

__device__ __forceinline__ void flag_set(LAS u8* L, int f, int w, int lane) {
  CBAR();
  if (lane == 0) *(volatile LAS u32*)(L + L_F + f * 16 + w * 4) = 1u;
  CBAR();
}
template <int SLEEP> __device__ __forceinline__ void flag_wait4(LAS u8* L, int f) {
#pragma nounroll
  for (int i = 0; i < SPIN_L; ++i) {
    v4u v = *(volatile LAS v4u*)(L + L_F + f * 16);
    if (__builtin_amdgcn_readfirstlane(v.x & v.y & v.z & v.w)) break;
    if (SLEEP) __builtin_amdgcn_s_sleep(SLEEP);
  }
  CBAR();
}
template <int SLEEP> __device__ __forceinline__ void flag_wait1(LAS u8* L, int f, int w) {
#pragma nounroll
  for (int i = 0; i < SPIN_L; ++i) {
    u32 v = *(volatile LAS u32*)(L + L_F + f * 16 + w * 4);
    if (__builtin_amdgcn_readfirstlane(v)) break;
    if (SLEEP) __builtin_amdgcn_s_sleep(SLEEP);
  }
  CBAR();
}
#define STAMP(i) do { } while (0)
#define STAMP_OUT() do { } while (0)
#define MKRSRC(p, n) __builtin_amdgcn_make_buffer_rsrc((void*)(p), 0, (int)(n), 0x00020000)

__global__ void __launch_bounds__(512, 2)
gqa_fused(const u8* x, const u8* kc, const u8* vc, const u8* wq, const u8* wo, u8* out, u8* ws) {
  extern __shared__ __attribute__((aligned(16))) u8 smem[];
  LAS u8* L = (LAS u8*)smem;
  const int tid = threadIdx.x, lane = tid & 63, wave = __builtin_amdgcn_readfirstlane(tid >> 6);
  const int g = lane >> 4, c = lane & 15;
  const int bid = blockIdx.x, kvh = bid & 7, j = bid >> 3, b = j >> 2, split = j & 3;
  const int wg = kvh * 32 + j;
  const int bh = j >> 4, hl = (j >> 2) & 3, nq = j & 3;
  STAMP(0);

  if (wave < 4) {
    const int w = wave;
    const int r8 = (w + 4 * (j & 1) + (j >> 1)) & 7;
    const size_t kvoff = ((size_t)((b * 8 + kvh) * 2048 + split * 512 + w * 128)) * 256;
    const u8* kb = kc + kvoff;
    const __amdgpu_buffer_rsrc_t rv = MKRSRC(vc + kvoff, 32768);
    u32 ko[4];
#pragma unroll
    for (int i = 0; i < 4; ++i) ko[i] = g * 256 + ((c ^ (4 * g + i)) * 16);
    const u32 ldsk = (u32)(size_t)L + L_K + w * 32768;
#pragma unroll
    for (int q4 = 0; q4 < 8; ++q4) {
#pragma unroll
      for (int i = 0; i < 4; ++i) {
        const int n = q4 * 4 + i;
        if (n % BURST == 0 && n + BURST > THR) WAITVM(THR - BURST);
        u32 keep;
        asm volatile("s_mov_b32 %0, m0\n\ts_mov_b32 m0, %3\n\ts_nop 0\n\tglobal_load_lds_dwordx4 %1, %2 offset:%4 nt\n\ts_mov_b32 m0, %0"
                     : "=&s"(keep) : "v"(ko[i]), "s"(kb + ((q4 + r8) & 7) * 4096), "s"(ldsk + q4 * 4096), "i"(i * 1024) : "memory");
        if (n == QTRIG) flag_set(L, F_K2, w, lane);
        if (n == LFIRST - 1) {
          if (w == 0) {
            *(volatile LAS u32*)(L + L_F + lane * 4) = 0u;
            asm volatile("s_waitcnt lgkmcnt(0)" ::: "memory");
          }
          SBAR();
          __builtin_amdgcn_s_barrier();
          SBAR();
        }
      }
    }
    v4u v[32];
#pragma unroll
    for (int i = 0; i < 32; ++i) {
      if (i % BURST == 0) {
        WAITVM(THR - BURST);
        if (i >= THR - BURST && i - BURST < THR - BURST) { flag_set(L, F_K, w, lane); STAMP(1); }
      }
      v[i] = __builtin_amdgcn_raw_buffer_load_b128(rv, lane * 16 + (i & 3) * 1024, (((i >> 2) + r8) & 7) * 4096, 2);
    }
    SBAR();
    flag_set(L, F_V, w, lane);
    STAMP(2);
    const __amdgpu_buffer_rsrc_t rs = MKRSRC(ws, WS_END);
    flag_wait4<1>(L, F_P);
    __builtin_amdgcn_s_setprio(3);
    v4u p[4];
#pragma unroll
    for (int t = 0; t < 4; ++t) p[t] = *(LAS v4u*)(L + L_P + w * 4096 + t * 1024 + lane * 16);
    const u32 tag = __builtin_amdgcn_readfirstlane(*(LAS u32*)(L + L_T));
    v4u stv;
    {
      const f4 mm = *(LAS f4*)(L + L_M + (4 * w + (lane & 3)) * 16), ll = *(LAS f4*)(L + L_L + (4 * w + (lane & 3)) * 16);
      stv = v4u{bf(fmaxf(fmaxf(mm[0], mm[1]), fmaxf(mm[2], mm[3])) * SCL), tag, bf((ll[0] + ll[1]) + (ll[2] + ll[3])), tag};
    }
    const u32 obase = WS_BOX + (kvh * 32 + b * 4 + w) * BOX + 8192;
    u32 ooff = obase + split * 2048 + lane * 16;
    u32 soff = lane < 4 ? obase + 8192 + split * 128 + lane * 16 : 0x7FFFFFF0u;
    u32 rsrc_ = L_K + ((lane >> 2) & 3) * 2112 + ((lane >> 4) & 1) * 1024 + ((lane >> 5) * 16 + 4 * w + (lane & 3)) * 16;
    u32 wdst_ = L_K + w * 32768 + lane * 16;
    PIN(rsrc_); PIN(wdst_); PIN(ooff); PIN(soff); PIN(stv);
    STAMP(6);
    f4 o[8];
#pragma unroll
    for (int e = 0; e < 8; ++e) o[e] = f4{0.f, 0.f, 0.f, 0.f};
#pragma unroll
    for (int t = 0; t < 4; ++t) {
#pragma unroll
      for (int e = 0; e < 8; ++e) o[e] = mfma(tr8(&v[8 * t], e), p[t], o[e]);
      if (t < 3) SBAR();
    }
    STAMP(3);
#pragma unroll
    for (int e = 0; e < 8; ++e) *(LAS f4*)(L + wdst_ + (e >> 1) * 2112 + (e & 1) * 1024) = o[e];
    flag_set(L, F_O, w, lane);
    flag_wait4<0>(L, F_O);
    {
      LAS u8* src = L + rsrc_;
      f4 s0 = *(LAS f4*)src, s1 = *(LAS f4*)(src + 512);
#pragma unroll
      for (int ww = 1; ww < 4; ++ww) {
        s0 += *(LAS f4*)(src + ww * 32768);
        s1 += *(LAS f4*)(src + ww * 32768 + 512);
      }
      __builtin_amdgcn_raw_buffer_store_b128(v4u{pk(s0[0], s0[1]), tag, pk(s0[2], s0[3]), tag}, rs, ooff, 0, 16);
      __builtin_amdgcn_raw_buffer_store_b128(v4u{pk(s1[0], s1[1]), tag, pk(s1[2], s1[3]), tag}, rs, ooff, 1024, 16);
      SBAR();
      __builtin_amdgcn_raw_buffer_store_b128(stv, rs, soff, 0, 16);
    }
    STAMP(4);
    if (w == 0) {
      __builtin_amdgcn_s_setprio(0);
      const u32 base = WS_BOX + wg * BOX + lane * 16;
      const int u = kvh * 4 + hl, row16 = u >> 1, eh = u & 1;
      u32 oo = ((bh * 16 + row16) * 512 + nq * 128 + 8 * c + 4 * eh + g) * 2;
      PIN(oo);
      v4u r[8];
#pragma unroll
      for (int k = 0; k < 8; ++k) r[k] = v4u{0u, 0u, 0u, 0u};
      u32 todo = 255u;
      flag_wait4<1>(L, F_A);
      __builtin_amdgcn_s_setprio(3);
      __builtin_amdgcn_s_sleep(FDELAY);
      f4 s;
#pragma nounroll
      for (int it = 0; it < SPIN_G; ++it) {
        fetch8(r, (u32)__builtin_amdgcn_readfirstlane(todo), ws, base, base + 4096);
        u32 bk[8];
#pragma unroll
        for (int k = 0; k < 8; ++k) bk[k] = orxor(r[k].y ^ tag, r[k].w, tag);
        const u32 bad = ((bk[0] | bk[1]) | (bk[2] | bk[3] | bk[4])) | (bk[5] | bk[6] | bk[7]);
        f4 h[4];
#pragma unroll
        for (int k = 0; k < 4; ++k)
          h[k] = f4{fb(r[2 * k].x << 16), fb(r[2 * k].x & 0xFFFF0000u), fb(r[2 * k].z << 16), fb(r[2 * k].z & 0xFFFF0000u)} +
                 f4{fb(r[2 * k + 1].x << 16), fb(r[2 * k + 1].x & 0xFFFF0000u), fb(r[2 * k + 1].z << 16), fb(r[2 * k + 1].z & 0xFFFF0000u)};
        s = (h[0] + h[1]) + (h[2] + h[3]);
        PIN(s);
        if (!__builtin_amdgcn_ballot_w64(bad != 0u)) break;
        u32 nt = 0;
#pragma unroll
        for (int k = 0; k < 8; ++k) nt |= __builtin_amdgcn_ballot_w64(bk[k] != 0u) ? (1u << k) : 0u;
        todo = nt;
      }
      const float tot = rsum4(s);
      const __bf16 ob = (__bf16)tot;
      *(GAS unsigned short*)(out + oo) = __builtin_bit_cast(unsigned short, ob);
      if ((bid & 127) == 0 && lane == 0) __builtin_amdgcn_raw_buffer_store_b32(tag, rs, WS_EPOCH + bh * 256, 0, 16);
      STAMP(5);
    }
    STAMP_OUT();
    asm volatile("gqa_code_mark:" ::: "memory");
    return;
  }

  const int cw = wave - 4, ct = tid - 256;
  if (cw == 3) {
    u64 pc;
    int len;
    asm volatile("s_getpc_b64 %0\n1:\n\ts_mov_b32 %1, gqa_code_mark-1b" : "=s"(pc), "=s"(len));
    const u32 lim = (u32)(len < 16 ? 16 : len) - 16u;
    u32 keep;
    asm volatile("s_mov_b32 %0, m0\n\ts_mov_b32 m0, %3\n\ts_nop 0\n\tglobal_load_lds_dwordx4 %1, %2\n\ts_mov_b32 m0, %0"
                 : "=&s"(keep) : "v"(min((u32)((j & 15) * 1024 + lane * 16), lim) & ~15u), "s"((const u8*)(pc & ~127ull)), "s"((u32)(size_t)L + L_Q) : "memory");
  }
  const __amdgpu_buffer_rsrc_t rs = MKRSRC(ws, WS_END);
  __builtin_amdgcn_s_setprio(3);
  const int qi = j & 7;
  const int qrot = (j >> 3) * 4;
  u32 tag;
  v4u wv[16];
  {
    v4u xa[2];
    const u32 ep = __builtin_amdgcn_raw_buffer_load_b32(rs, ct == 0 ? WS_EPOCH + bh * 256 : 0x7FFFFFF0u, 0, 16);
    const __amdgpu_buffer_rsrc_t rx = MKRSRC(x, 32768);
    const __amdgpu_buffer_rsrc_t rq = MKRSRC(wq, 4u << 20);
    const u32 xo = (((b & ~1) * 4 + (c & 7)) * 512 + cw * 128 + (2 * g + (c >> 3)) * 8) * 2;
#pragma unroll
    for (int T = 0; T < 2; ++T) xa[T] = __builtin_amdgcn_raw_buffer_load_b128(rx, xo, (T ^ (qrot >> 3)) * 128, 0);
    SBAR();
    const u32 wqo = ((cw * 128 + (lane >> 3) * 8) * 4096 + kvh * 512 + qi * 64 + (lane & 7) * 8) * 2;
#pragma unroll
    for (int i = 0; i < 16; ++i) {
      const int ii = i ^ qrot;
      wv[i] = __builtin_amdgcn_raw_buffer_load_b128(rq, wqo, ((ii >> 3) * 64 + (ii & 7)) * 8192, 0);
      if (i == CFIRST - 1) { SBAR(); __builtin_amdgcn_s_barrier(); SBAR(); }
    }
    SBAR();
    STAMP(11);
    if (qrot & 4) {
#pragma unroll
      for (int T = 0; T < 2; ++T) xa[T] = v4u{xa[T].z, xa[T].w, xa[T].x, xa[T].y};
    }
    f4 qa[8];
#pragma unroll
    for (int e = 0; e < 8; ++e) qa[e] = f4{0.f, 0.f, 0.f, 0.f};
#pragma unroll
    for (int T = 0; T < 2; ++T)
#pragma unroll
      for (int e = 0; e < 8; ++e) qa[e] = mfma(xa[T], tr8(&wv[8 * T], e), qa[e]);
    if ((g >> 1) == (c >> 3)) {
#pragma unroll
      for (int r = 0; r < 4; ++r) {
        LAS u8* dst = L + L_QP + cw * 4096 + (g * 4 + r) * 256 + (c & 7) * 32;
        *(LAS f4*)dst = f4{qa[0][r], qa[1][r], qa[2][r], qa[3][r]};
        *(LAS f4*)(dst + 16) = f4{qa[4][r], qa[5][r], qa[6][r], qa[7][r]};
      }
    }
    if (ct == 0) *(LAS u32*)(L + L_T) = ep + 1u;
    flag_set(L, F_QP, cw, lane);
    flag_wait4<0>(L, F_QP);
    tag = __builtin_amdgcn_readfirstlane(*(LAS u32*)(L + L_T));
    if (cw < 2) {
      const int row = ct >> 4;
      f4 s = {0.f, 0.f, 0.f, 0.f};
#pragma unroll
      for (int ww = 0; ww < 8; ++ww) s += *(LAS f4*)(L + L_QP + ww * 2048 + row * 256 + c * 16);
      const u32 off = WS_QBUF + ((((b & ~1) + (row >> 2)) * 8 + kvh) * 16 + (qi >> 1) * 4 + (row & 3)) * 512 + (qi & 1) * 256 + c * 16;
      __builtin_amdgcn_raw_buffer_store_b128(v4u{pk(s[0], s[1]), tag, pk(s[2], s[3]), tag}, rs, off, 0, 16);
    }
    SBAR();
    STAMP(2);
  }
  {
    const __amdgpu_buffer_rsrc_t ro = MKRSRC(wo + (size_t)(kvh * 4 + hl) * 131072, 131072);
    const u32 woo = (cw * 32 + 2 * g) * 1024 + nq * 256 + c * 16;
#pragma unroll
    for (int i = 0; i < 8; ++i) wv[i] = __builtin_amdgcn_raw_buffer_load_b128(ro, woo, ((i & 3) * 8 + ((i >> 2) ^ bh)) * 1024, 0);
    SBAR();
  }
  flag_wait1<4>(L, F_K2, cw);
  STAMP(1);
  {
    const u32 base = WS_QBUF + ((b * 8 + kvh) * 16 + cw * 4) * 512 + lane * 16;
    v4u r[2] = {v4u{0u, 0u, 0u, 0u}, v4u{0u, 0u, 0u, 0u}};
    u64 m0 = ~0ull, m1 = ~0ull;
#pragma nounroll
    for (int it = 0; it < SPIN_G; ++it) {
      poll2(r, m0, m1, ws, base);
      m0 = stale(r[0], tag);
      m1 = stale(r[1], tag);
      if (!(m0 | m1)) break;
    }
    LAS u8* dst = L + L_Q + (((lane >> 3) & 3) * 64 + ((lane >> 1) & 3) * 16 + cw * 4 + (lane >> 5)) * 16 + (lane & 1) * 8;
    *(LAS v2u*)dst = v2u{r[0].x, r[0].z};
    *(LAS v2u*)(dst + 32) = v2u{r[1].x, r[1].z};
    STAMP(3);
    flag_set(L, F_Q, cw, lane);
    flag_wait4<1>(L, F_Q);
  }
  v4u qf[4];
#pragma unroll
  for (int s = 0; s < 4; ++s) qf[s] = *(LAS v4u*)(L + L_Q + s * 1024 + lane * 16);
  flag_wait1<1>(L, F_K, cw);
  STAMP(4);
  f4 sc[8];
  {
    LAS u8* kr = L + L_K + cw * 32768 + (4 * (c & 3) + (c >> 2)) * 256;
    v4u kf[32];
#pragma unroll
    for (int u = 0; u < 8; ++u)
#pragma unroll
      for (int s = 0; s < 4; ++s) kf[u * 4 + s] = *(LAS v4u*)(kr + u * 4096 + ((4 * s + g) ^ c) * 16);
    SBAR();
#pragma unroll
    for (int u = 0; u < 8; ++u) {
      sc[u] = f4{0.f, 0.f, 0.f, 0.f};
#pragma unroll
      for (int s = 0; s < 4; ++s) sc[u] = mfma(kf[u * 4 + s], qf[s], sc[u]);
    }
  }
  {
    float m = sc[0][0];
#pragma unroll
    for (int u = 0; u < 8; ++u)
#pragma unroll
      for (int r = 0; r < 4; ++r) m = fmaxf(m, sc[u][r]);
    m = xmax(m);
    if (lane < 16) *(LAS float*)(L + L_M + c * 16 + cw * 4) = m;
    flag_set(L, F_M, cw, lane);
#pragma unroll
    for (int e = 0; e < 8; ++e) {
      const v4u fr = tr8(wv, e);
      *(LAS v4u*)(L + L_K + cw * 32768 + 16384 + e * 1024 + lane * 16) = bh ? v4u{fr.z, fr.x, fr.w, fr.y} : v4u{fr.x, fr.z, fr.y, fr.w};
    }
    flag_wait4<0>(L, F_M);
    const f4 mm = *(LAS f4*)(L + L_M + c * 16);
    const float M = fmaxf(fmaxf(mm[0], mm[1]), fmaxf(mm[2], mm[3])) * SCL;
    float l = 0.f;
#pragma unroll
    for (int u = 0; u < 8; ++u)
#pragma unroll
      for (int r = 0; r < 4; ++r) {
        sc[u][r] = __builtin_amdgcn_exp2f(__builtin_fmaf(sc[u][r], SCL, -M));
        l += sc[u][r];
      }
    l = xsum(l);
    if (lane < 16) *(LAS float*)(L + L_L + c * 16 + cw * 4) = l;
#pragma unroll
    for (int t = 0; t < 4; ++t) {
      const v4u pv = {pk(sc[2 * t][0], sc[2 * t][1]), pk(sc[2 * t][2], sc[2 * t][3]), pk(sc[2 * t + 1][0], sc[2 * t + 1][1]), pk(sc[2 * t + 1][2], sc[2 * t + 1][3])};
      *(LAS v4u*)(L + L_P + cw * 4096 + t * 1024 + lane * 16) = pv;
    }
    flag_set(L, F_P, cw, lane);
    STAMP(5);
  }
  __builtin_amdgcn_s_setprio(0);
  flag_wait4<1>(L, F_P);
  v4u wf[32];
#pragma unroll
  for (int t = 0; t < 4; ++t)
#pragma unroll
    for (int e = 0; e < 8; ++e) wf[t * 8 + e] = *(LAS v4u*)(L + L_K + t * 32768 + 16384 + e * 1024 + lane * 16);
#pragma unroll
  for (int i = 0; i < 32; ++i) PIN(wf[i]);
  STAMP(6);
  {
    const u32 obase = WS_BOX + (kvh * 32 + (bh * 4 + cw) * 4 + hl) * BOX + 8192;
    u32 v0 = obase + (c >> 2) * 2048 + g * 64 + (c & 3) * 16;
    const u32 vs = obase + 8192 + g * 128 + (c & 3) * 16;
    const int u = (cw * 4 + g) * 2;
    u32 off = WS_BOX + ((u >> 2) * 32 + bh * 16 + (u & 3) * 4 + nq) * BOX + (kvh * 4 + hl) * 256 + c * 16;
    PIN(off); PIN(v0);
    flag_wait4<2>(L, F_V);
    STAMP(8);
    v4u st;
#pragma nounroll
    for (int it = 0; it < SPIN_G; ++it) {
      ld1(st, ws, vs);
      if (!stale(st, tag)) break;
      __builtin_amdgcn_s_sleep(1);
    }
    __builtin_amdgcn_s_setprio(3);
    STAMP(7);
    float res[8];
#pragma nounroll
    for (int it = 0; it < SPIN_G; ++it) {
      v4u r[8];
      ld8_issue(r, ws, v0);
      SBAR();
      const float mx = xmax(fb(st.x));
      const float ex = __builtin_amdgcn_exp2f(fb(st.x) - mx);
      const float fq = ex * __builtin_amdgcn_rcpf(xsum(ex * fb(st.z)));
      f4 f = {dppf<0x00>(fq), dppf<0x55>(fq), dppf<0xAA>(fq), dppf<0xFF>(fq)};
      PIN(f);
      ld8_wait(r);
      STAMP(9);
      u32 bad = 0;
#pragma unroll
      for (int t = 0; t < 4; ++t) {
        bad = orxor(orxor(bad, r[2 * t].y, tag), r[2 * t].w, tag);
        r[2 * t].y = r[2 * t + 1].x;
        r[2 * t].w = r[2 * t + 1].z;
      }
#pragma unroll
      for (int e = 0; e < 8; ++e) {
        f4 d = {0.f, 0.f, 0.f, 0.f};
#pragma unroll
        for (int t = 0; t < 4; ++t) d = mfma(r[2 * t], wf[t * 8 + e], d);
        if (e < 4) bad = orxor(orxor(bad, r[2 * e + 1].y, tag), r[2 * e + 1].w, tag);
        res[e] = rsum4(d * f);
      }
      if (!__builtin_amdgcn_ballot_w64(bad != 0u)) break;
    }
    __builtin_amdgcn_raw_buffer_store_b128(v4u{pk(res[0], res[1]), tag, pk(res[2], res[3]), tag}, rs, off, 0, 16);
    __builtin_amdgcn_raw_buffer_store_b128(v4u{pk(res[4], res[5]), tag, pk(res[6], res[7]), tag}, rs, off, 4 * BOX, 16);
    flag_set(L, F_A, cw, lane);
    STAMP(10);
  }
  STAMP_OUT();
}

extern "C" void kernel_launch(void* const* d_in, const int* in_sizes, int n_in, void* d_out, int out_size, void* d_ws,
                              size_t ws_size, hipStream_t stream) {
  (void)in_sizes; (void)n_in; (void)out_size; (void)ws_size;
  (void)hipFuncSetAttribute((const void*)gqa_fused, hipFuncAttributeMaxDynamicSharedMemorySize, (int)LDS_BYTES);
  hipLaunchKernelGGL(gqa_fused, dim3(256), dim3(512), LDS_BYTES, stream, (const u8*)d_in[0], (const u8*)d_in[1], (const u8*)d_in[2],
                     (const u8*)d_in[3], (const u8*)d_in[4], (u8*)d_out, (u8*)d_ws);
}
```

```cpp
#include <hip/hip_runtime.h>
#include <stdint.h>

typedef unsigned u32;
typedef unsigned long long u64;
typedef unsigned char u8;
typedef u32 v4u __attribute__((ext_vector_type(4)));
typedef u32 v2u __attribute__((ext_vector_type(2)));
typedef float f4 __attribute__((ext_vector_type(4)));
typedef float f2 __attribute__((ext_vector_type(2)));
typedef short s8v __attribute__((ext_vector_type(8)));
typedef __bf16 bf2v __attribute__((ext_vector_type(2)));
#define GAS __attribute__((address_space(1)))
#define LAS __attribute__((address_space(3)))

#define THR 12
#define BURST 4
#define LFIRST 4
#define CFIRST 8
#define QTRIG 28
#define PEND 940
#define PDELTA 138
#define PSTEP BURST
#define FDELAY 12
#define CODEPF 1

constexpr u32 WS_EPOCH = 0;
constexpr u32 WS_QBUF = 4096;
constexpr u32 WS_BOX = WS_QBUF + 64 * 8192;
constexpr u32 BOX = 16896;
constexpr u32 WS_END = WS_BOX + 256 * BOX;
constexpr u32 L_K = 0;
constexpr u32 L_P = 131072;
constexpr u32 L_QP = L_P, L_Q = L_P + 16384;
constexpr u32 L_M = L_Q + 4096, L_L = L_M + 256, L_T = L_L + 256, L_F = L_T + 64;
constexpr u32 LDS_BYTES = L_F + 256;
enum { F_QP = 0, F_Q, F_K, F_M, F_P, F_O, F_V, F_A, F_K2, F_W };

constexpr int SPIN_G = 1 << 17;
constexpr int SPIN_L = 1 << 22;
constexpr float SCL = 0.08838834764831845f * 1.4426950408889634f;

__device__ __forceinline__ f4 mfma(v4u a, v4u b, f4 c) {
  return __builtin_amdgcn_mfma_f32_16x16x32_bf16(__builtin_bit_cast(s8v, a), __builtin_bit_cast(s8v, b), c, 0, 0, 0);
}
__device__ __forceinline__ u32 pk(float lo, float hi) {
  f2 v = {lo, hi};
  return __builtin_bit_cast(u32, __builtin_convertvector(v, bf2v));
}
__device__ __forceinline__ u32 plo(u32 a, u32 b) { return __builtin_amdgcn_perm(b, a, 0x05040100u); }
__device__ __forceinline__ u32 phi(u32 a, u32 b) { return __builtin_amdgcn_perm(b, a, 0x07060302u); }
__device__ __forceinline__ v4u tr8(const v4u* v, int e) {
  v4u r;
#pragma unroll
  for (int jj = 0; jj < 4; ++jj) {
    const u32 a0 = v[2 * jj][e >> 1], a1 = v[2 * jj + 1][e >> 1];
    r[jj] = (e & 1) ? phi(a0, a1) : plo(a0, a1);
  }
  return r;
}
__device__ __forceinline__ float fb(u32 x) { return __builtin_bit_cast(float, x); }
__device__ __forceinline__ u32 bf(float x) { return __builtin_bit_cast(u32, x); }
__device__ __forceinline__ float xmax(float x) {
  auto s = __builtin_amdgcn_permlane16_swap(bf(x), bf(x), false, false);
  x = fmaxf(fb(s[0]), fb(s[1]));
  auto t = __builtin_amdgcn_permlane32_swap(bf(x), bf(x), false, false);
  return fmaxf(fb(t[0]), fb(t[1]));
}
__device__ __forceinline__ float xsum(float x) {
  auto s = __builtin_amdgcn_permlane16_swap(bf(x), bf(x), false, false);
  x = fb(s[0]) + fb(s[1]);
  auto t = __builtin_amdgcn_permlane32_swap(bf(x), bf(x), false, false);
  return fb(t[0]) + fb(t[1]);
}
__device__ __forceinline__ float rsum4(f4 d) {
  auto s02 = __builtin_amdgcn_permlane32_swap(bf(d[0]), bf(d[2]), false, false);
  auto s13 = __builtin_amdgcn_permlane32_swap(bf(d[1]), bf(d[3]), false, false);
  const float x02 = fb(s02[0]) + fb(s02[1]), x13 = fb(s13[0]) + fb(s13[1]);
  auto s = __builtin_amdgcn_permlane16_swap(bf(x02), bf(x13), false, false);
  return fb(s[0]) + fb(s[1]);
}
template <int CTRL> __device__ __forceinline__ float dppf(float x) {
  return __builtin_bit_cast(float, __builtin_amdgcn_mov_dpp(__builtin_bit_cast(int, x), CTRL, 0xf, 0xf, true));
}
__device__ __forceinline__ u64 stale(v4u r, u32 tag) { return __builtin_amdgcn_ballot_w64(((r.y ^ tag) | (r.w ^ tag)) != 0u); }
__device__ __forceinline__ u32 orxor(u32 acc, u32 a, u32 t) { return __builtin_amdgcn_bitop3_b32(acc, a, t, 0xF6); }

__device__ __forceinline__ void poll2(v4u (&r)[2], u64 m0, u64 m1, const u8* base, u32 voff) {
  u64 sv;
  asm volatile("s_mov_b64 %2, exec\n\t"
               "s_mov_b64 exec, %5\n\tglobal_load_dwordx4 %0, %3, %4 sc1\n\t"
               "s_mov_b64 exec, %6\n\tglobal_load_dwordx4 %1, %3, %4 offset:1024 sc1\n\t"
               "s_mov_b64 exec, %2\n\ts_waitcnt vmcnt(0)"
               : "+v"(r[0]), "+v"(r[1]), "=&s"(sv) : "v"(voff), "s"(base), "s"(m0), "s"(m1) : "memory");
}
__device__ __forceinline__ void ld8_issue(v4u (&r)[8], const u8* base, u32 v0) {
  asm volatile("global_load_dwordx4 %0, %8, %9 sc1\n\tglobal_load_dwordx4 %1, %8, %9 offset:256 sc1\n\t"
               "global_load_dwordx4 %2, %8, %9 offset:512 sc1\n\tglobal_load_dwordx4 %3, %8, %9 offset:768 sc1\n\t"
               "global_load_dwordx4 %4, %8, %9 offset:1024 sc1\n\tglobal_load_dwordx4 %5, %8, %9 offset:1280 sc1\n\t"
               "global_load_dwordx4 %6, %8, %9 offset:1536 sc1\n\tglobal_load_dwordx4 %7, %8, %9 offset:1792 sc1"
               : "=&v"(r[0]), "=&v"(r[1]), "=&v"(r[2]), "=&v"(r[3]), "=&v"(r[4]), "=&v"(r[5]), "=&v"(r[6]), "=&v"(r[7])
               : "v"(v0), "s"(base) : "memory");
}
__device__ __forceinline__ void ld8_wait(v4u (&r)[8]) {
  asm volatile("s_waitcnt vmcnt(0)"
               : "+v"(r[0]), "+v"(r[1]), "+v"(r[2]), "+v"(r[3]), "+v"(r[4]), "+v"(r[5]), "+v"(r[6]), "+v"(r[7]) :: "memory");
}
__device__ __forceinline__ void fetch8(v4u (&r)[8], u32 want, const u8* base, u32 v0, u32 v1) {
  asm volatile("s_bitcmp1_b32 %11, 0\n\ts_cbranch_scc0 1f\n\tglobal_load_dwordx4 %0, %8, %10 sc1\n"
               "1:\n\ts_bitcmp1_b32 %11, 1\n\ts_cbranch_scc0 2f\n\tglobal_load_dwordx4 %1, %8, %10 offset:1024 sc1\n"
               "2:\n\ts_bitcmp1_b32 %11, 2\n\ts_cbranch_scc0 3f\n\tglobal_load_dwordx4 %2, %8, %10 offset:2048 sc1\n"
               "3:\n\ts_bitcmp1_b32 %11, 3\n\ts_cbranch_scc0 4f\n\tglobal_load_dwordx4 %3, %8, %10 offset:3072 sc1\n"
               "4:\n\ts_bitcmp1_b32 %11, 4\n\ts_cbranch_scc0 5f\n\tglobal_load_dwordx4 %4, %9, %10 sc1\n"
               "5:\n\ts_bitcmp1_b32 %11, 5\n\ts_cbranch_scc0 6f\n\tglobal_load_dwordx4 %5, %9, %10 offset:1024 sc1\n"
               "6:\n\ts_bitcmp1_b32 %11, 6\n\ts_cbranch_scc0 7f\n\tglobal_load_dwordx4 %6, %9, %10 offset:2048 sc1\n"
               "7:\n\ts_bitcmp1_b32 %11, 7\n\ts_cbranch_scc0 8f\n\tglobal_load_dwordx4 %7, %9, %10 offset:3072 sc1\n"
               "8:\n\ts_waitcnt vmcnt(0)"
               : "+v"(r[0]), "+v"(r[1]), "+v"(r[2]), "+v"(r[3]), "+v"(r[4]), "+v"(r[5]), "+v"(r[6]), "+v"(r[7])
               : "v"(v0), "v"(v1), "s"(base), "s"(want) : "memory", "scc");
}
__device__ __forceinline__ void ld1(v4u& r, const u8* base, u32 v0) {
  asm volatile("global_load_dwordx4 %0, %1, %2 sc1\n\ts_waitcnt vmcnt(0)" : "=&v"(r) : "v"(v0), "s"(base) : "memory");
}
#define CBAR() asm volatile("" ::: "memory")
#define SBAR() __builtin_amdgcn_sched_barrier(0)
#define PIN(x) asm volatile("" : "+v"(x))
#define WAITVM(n) asm volatile("s_waitcnt vmcnt(%0)" ::"i"(n) : "memory")

__device__ __forceinline__ void pace(u64 tz, int ticks) {
#pragma nounroll
  while ((int)(u32)(__builtin_amdgcn_s_memrealtime() - tz) < ticks) __builtin_amdgcn_s_sleep(2);
}
__device__ __forceinline__ void flag_set(LAS u8* L, int f, int w, int lane) {
  CBAR();
  if (lane == 0) *(volatile LAS u32*)(L + L_F + f * 16 + w * 4) = 1u;
  CBAR();
}
template <int SLEEP> __device__ __forceinline__ void flag_wait4(LAS u8* L, int f) {
#pragma nounroll
  for (int i = 0; i < SPIN_L; ++i) {
    v4u v = *(volatile LAS v4u*)(L + L_F + f * 16);
    if (__builtin_amdgcn_readfirstlane(v.x & v.y & v.z & v.w)) break;
    if (SLEEP) __builtin_amdgcn_s_sleep(SLEEP);
  }
  CBAR();
}
template <int SLEEP> __device__ __forceinline__ void flag_wait1(LAS u8* L, int f, int w) {
#pragma nounroll
  for (int i = 0; i < SPIN_L; ++i) {
    u32 v = *(volatile LAS u32*)(L + L_F + f * 16 + w * 4);
    if (__builtin_amdgcn_readfirstlane(v)) break;
    if (SLEEP) __builtin_amdgcn_s_sleep(SLEEP);
  }
  CBAR();
}
#define MKRSRC(p, n) __builtin_amdgcn_make_buffer_rsrc((void*)(p), 0, (int)(n), 0x00020000)

__global__ void __launch_bounds__(512, 2)
gqa_fused(const u8* x, const u8* kc, const u8* vc, const u8* wq, const u8* wo, u8* out, u8* ws) {
  extern __shared__ __attribute__((aligned(16))) u8 smem[];
  LAS u8* L = (LAS u8*)smem;
  const int tid = threadIdx.x, lane = tid & 63, wave = __builtin_amdgcn_readfirstlane(tid >> 6);
  const int g = lane >> 4, c = lane & 15;
  const int bid = blockIdx.x, kvh = bid & 7, j = bid >> 3, b = j >> 2, split = j & 3;
  const int wg = kvh * 32 + j;
  const int bh = j >> 4, hl = (j >> 2) & 3, nq = j & 3;

  if (wave < 4) {
    const int w = wave;
    const u64 tz = __builtin_amdgcn_s_memrealtime();
    const int r8 = (w + 4 * (j & 1) + (j >> 1)) & 7;
    const size_t kvoff = ((size_t)((b * 8 + kvh) * 2048 + split * 512 + w * 128)) * 256;
    const u8* kb = kc + kvoff;
    const __amdgpu_buffer_rsrc_t rv = MKRSRC(vc + kvoff, 32768);
    u32 ko[4];
#pragma unroll
    for (int i = 0; i < 4; ++i) ko[i] = g * 256 + ((c ^ (4 * g + i)) * 16);
    const u32 ldsk = (u32)(size_t)L + L_K + w * 32768;
#pragma unroll
    for (int q4 = 0; q4 < 8; ++q4) {
#pragma unroll
      for (int i = 0; i < 4; ++i) {
        const int n = q4 * 4 + i;
        if (PEND && n % PSTEP == 0 && n >= 16) pace(tz, PEND - (63 - n) * PDELTA / 10);
        if (n % BURST == 0 && n + BURST > THR) WAITVM(THR - BURST);
        u32 keep;
        asm volatile("s_mov_b32 %0, m0\n\ts_mov_b32 m0, %3\n\ts_nop 0\n\tglobal_load_lds_dwordx4 %1, %2 offset:%4 nt\n\ts_mov_b32 m0, %0"
                     : "=&s"(keep) : "v"(ko[i]), "s"(kb + ((q4 + r8) & 7) * 4096), "s"(ldsk + q4 * 4096), "i"(i * 1024) : "memory");
        if (n == QTRIG) flag_set(L, F_K2, w, lane);
        if (n == LFIRST - 1) {
          if (w == 0) {
            *(volatile LAS u32*)(L + L_F + lane * 4) = 0u;
            asm volatile("s_waitcnt lgkmcnt(0)" ::: "memory");
          }
          SBAR();
          __builtin_amdgcn_s_barrier();
          SBAR();
        }
      }
    }
    v4u v[32];
#pragma unroll
    for (int i = 0; i < 32; ++i) {
      if (PEND && i % PSTEP == 0) pace(tz, PEND - (31 - i) * PDELTA / 10);
      if (i % BURST == 0) {
        WAITVM(THR - BURST);
        if (i >= THR - BURST && i - BURST < THR - BURST) { flag_set(L, F_K, w, lane); }
      }
      v[i] = __builtin_amdgcn_raw_buffer_load_b128(rv, lane * 16 + (i & 3) * 1024, (((i >> 2) + r8) & 7) * 4096, 2);
    }
    SBAR();
    flag_set(L, F_V, w, lane);
    const __amdgpu_buffer_rsrc_t rs = MKRSRC(ws, WS_END);
    flag_wait4<1>(L, F_P);
    __builtin_amdgcn_s_setprio(3);
    v4u p[4];
#pragma unroll
    for (int t = 0; t < 4; ++t) p[t] = *(LAS v4u*)(L + L_P + w * 4096 + t * 1024 + lane * 16);
    const u32 tag = __builtin_amdgcn_readfirstlane(*(LAS u32*)(L + L_T));
    v4u stv;
    {
      const f4 mm = *(LAS f4*)(L + L_M + (4 * w + (lane & 3)) * 16), ll = *(LAS f4*)(L + L_L + (4 * w + (lane & 3)) * 16);
      stv = v4u{bf(fmaxf(fmaxf(mm[0], mm[1]), fmaxf(mm[2], mm[3])) * SCL), tag, bf((ll[0] + ll[1]) + (ll[2] + ll[3])), tag};
    }
    const u32 obase = WS_BOX + (kvh * 32 + b * 4 + w) * BOX + 8192;
    u32 ooff = obase + split * 2048 + lane * 16;
    u32 soff = lane < 4 ? obase + 8192 + split * 128 + lane * 16 : 0x7FFFFFF0u;
    u32 rsrc_ = L_K + ((lane >> 2) & 3) * 2112 + ((lane >> 4) & 1) * 1024 + ((lane >> 5) * 16 + 4 * w + (lane & 3)) * 16;
    u32 wdst_ = L_K + w * 32768 + lane * 16;
    PIN(rsrc_); PIN(wdst_); PIN(ooff); PIN(soff); PIN(stv);
    f4 o[8];
#pragma unroll
    for (int e = 0; e < 8; ++e) o[e] = f4{0.f, 0.f, 0.f, 0.f};
#pragma unroll
    for (int t = 0; t < 4; ++t) {
#pragma unroll
      for (int e = 0; e < 8; ++e) o[e] = mfma(tr8(&v[8 * t], e), p[t], o[e]);
      if (t < 3) SBAR();
    }
#pragma unroll
    for (int e = 0; e < 8; ++e) *(LAS f4*)(L + wdst_ + (e >> 1) * 2112 + (e & 1) * 1024) = o[e];
    flag_set(L, F_O, w, lane);
    flag_wait4<0>(L, F_O);
    {
      LAS u8* src = L + rsrc_;
      f4 s0 = *(LAS f4*)src, s1 = *(LAS f4*)(src + 512);
#pragma unroll
      for (int ww = 1; ww < 4; ++ww) {
        s0 += *(LAS f4*)(src + ww * 32768);
        s1 += *(LAS f4*)(src + ww * 32768 + 512);
      }
      __builtin_amdgcn_raw_buffer_store_b128(v4u{pk(s0[0], s0[1]), tag, pk(s0[2], s0[3]), tag}, rs, ooff, 0, 16);
      __builtin_amdgcn_raw_buffer_store_b128(v4u{pk(s1[0], s1[1]), tag, pk(s1[2], s1[3]), tag}, rs, ooff, 1024, 16);
      SBAR();
      __builtin_amdgcn_raw_buffer_store_b128(stv, rs, soff, 0, 16);
    }
    if (w == 0) {
      __builtin_amdgcn_s_setprio(0);
      const u32 base = WS_BOX + wg * BOX + lane * 16;
      const int u = kvh * 4 + hl, row16 = u >> 1, eh = u & 1;
      u32 oo = ((bh * 16 + row16) * 512 + nq * 128 + 8 * c + 4 * eh + g) * 2;
      PIN(oo);
      v4u r[8];
#pragma unroll
      for (int k = 0; k < 8; ++k) r[k] = v4u{0u, 0u, 0u, 0u};
      u32 todo = 255u;
      flag_wait4<1>(L, F_A);
      __builtin_amdgcn_s_setprio(3);
      __builtin_amdgcn_s_sleep(FDELAY);
      f4 s;
#pragma nounroll
      for (int it = 0; it < SPIN_G; ++it) {
        fetch8(r, (u32)__builtin_amdgcn_readfirstlane(todo), ws, base, base + 4096);
        u32 bk[8];
#pragma unroll
        for (int k = 0; k < 8; ++k) bk[k] = orxor(r[k].y ^ tag, r[k].w, tag);
        const u32 bad = ((bk[0] | bk[1]) | (bk[2] | bk[3] | bk[4])) | (bk[5] | bk[6] | bk[7]);
        f4 h[4];
#pragma unroll
        for (int k = 0; k < 4; ++k)
          h[k] = f4{fb(r[2 * k].x << 16), fb(r[2 * k].x & 0xFFFF0000u), fb(r[2 * k].z << 16), fb(r[2 * k].z & 0xFFFF0000u)} +
                 f4{fb(r[2 * k + 1].x << 16), fb(r[2 * k + 1].x & 0xFFFF0000u), fb(r[2 * k + 1].z << 16), fb(r[2 * k + 1].z & 0xFFFF0000u)};
        s = (h[0] + h[1]) + (h[2] + h[3]);
        PIN(s);
        if (!__builtin_amdgcn_ballot_w64(bad != 0u)) break;
        u32 nt = 0;
#pragma unroll
        for (int k = 0; k < 8; ++k) nt |= __builtin_amdgcn_ballot_w64(bk[k] != 0u) ? (1u << k) : 0u;
        todo = nt;
      }
      const float tot = rsum4(s);
      const __bf16 ob = (__bf16)tot;
      *(GAS unsigned short*)(out + oo) = __builtin_bit_cast(unsigned short, ob);
      if ((bid & 127) == 0 && lane == 0) __builtin_amdgcn_raw_buffer_store_b32(tag, rs, WS_EPOCH + bh * 256, 0, 16);
    }
    asm volatile("gqa_code_mark:" ::: "memory");
    return;
  }

  const int cw = wave - 4, ct = tid - 256;
  if (cw == 3) {
    u64 pc;
    int len;
    asm volatile("s_getpc_b64 %0\n1:\n\ts_mov_b32 %1, gqa_code_mark-1b" : "=s"(pc), "=s"(len));
    const u32 lim = (u32)(len < 16 ? 16 : len) - 16u;
    u32 keep;
    asm volatile("s_mov_b32 %0, m0\n\ts_mov_b32 m0, %3\n\ts_nop 0\n\tglobal_load_lds_dwordx4 %1, %2\n\ts_mov_b32 m0, %0"
                 : "=&s"(keep) : "v"(min((u32)((j & 15) * 1024 + lane * 16), lim) & ~15u), "s"((const u8*)(pc & ~127ull)), "s"((u32)(size_t)L + L_Q) : "memory");
  }
  const __amdgpu_buffer_rsrc_t rs = MKRSRC(ws, WS_END);
  __builtin_amdgcn_s_setprio(3);
  const int qi = j & 7;
  const int qrot = (j >> 3) * 4;
  u32 tag;
  v4u wv[16];
  {
    v4u xa[2];
    const u32 ep = __builtin_amdgcn_raw_buffer_load_b32(rs, ct == 0 ? WS_EPOCH + bh * 256 : 0x7FFFFFF0u, 0, 16);
    const __amdgpu_buffer_rsrc_t rx = MKRSRC(x, 32768);
    const __amdgpu_buffer_rsrc_t rq = MKRSRC(wq, 4u << 20);
    const u32 xo = (((b & ~1) * 4 + (c & 7)) * 512 + cw * 128 + (2 * g + (c >> 3)) * 8) * 2;
#pragma unroll
    for (int T = 0; T < 2; ++T) xa[T] = __builtin_amdgcn_raw_buffer_load_b128(rx, xo, (T ^ (qrot >> 3)) * 128, 0);
    SBAR();
    const u32 wqo = ((cw * 128 + (lane >> 3) * 8) * 4096 + kvh * 512 + qi * 64 + (lane & 7) * 8) * 2;
#pragma unroll
    for (int i = 0; i < 16; ++i) {
      const int ii = i ^ qrot;
      wv[i] = __builtin_amdgcn_raw_buffer_load_b128(rq, wqo, ((ii >> 3) * 64 + (ii & 7)) * 8192, 0);
      if (i == CFIRST - 1) { SBAR(); __builtin_amdgcn_s_barrier(); SBAR(); }
    }
    SBAR();
    if (qrot & 4) {
#pragma unroll
      for (int T = 0; T < 2; ++T) xa[T] = v4u{xa[T].z, xa[T].w, xa[T].x, xa[T].y};
    }
    f4 qa[8];
#pragma unroll
    for (int e = 0; e < 8; ++e) qa[e] = f4{0.f, 0.f, 0.f, 0.f};
#pragma unroll
    for (int T = 0; T < 2; ++T)
#pragma unroll
      for (int e = 0; e < 8; ++e) qa[e] = mfma(xa[T], tr8(&wv[8 * T], e), qa[e]);
    if ((g >> 1) == (c >> 3)) {
#pragma unroll
      for (int r = 0; r < 4; ++r) {
        LAS u8* dst = L + L_QP + cw * 4096 + (g * 4 + r) * 256 + (c & 7) * 32;
        *(LAS f4*)dst = f4{qa[0][r], qa[1][r], qa[2][r], qa[3][r]};
        *(LAS f4*)(dst + 16) = f4{qa[4][r], qa[5][r], qa[6][r], qa[7][r]};
      }
    }
    if (ct == 0) *(LAS u32*)(L + L_T) = ep + 1u;
    flag_set(L, F_QP, cw, lane);
    flag_wait4<0>(L, F_QP);
    tag = __builtin_amdgcn_readfirstlane(*(LAS u32*)(L + L_T));
    if (cw < 2) {
      const int row = ct >> 4;
      f4 s = {0.f, 0.f, 0.f, 0.f};
#pragma unroll
      for (int ww = 0; ww < 8; ++ww) s += *(LAS f4*)(L + L_QP + ww * 2048 + row * 256 + c * 16);
      const u32 off = WS_QBUF + ((((b & ~1) + (row >> 2)) * 8 + kvh) * 16 + (qi >> 1) * 4 + (row & 3)) * 512 + (qi & 1) * 256 + c * 16;
      __builtin_amdgcn_raw_buffer_store_b128(v4u{pk(s[0], s[1]), tag, pk(s[2], s[3]), tag}, rs, off, 0, 16);
    }
    SBAR();
  }
  flag_wait1<4>(L, F_K2, cw);
  {
    const u32 base = WS_QBUF + ((b * 8 + kvh) * 16 + cw * 4) * 512 + lane * 16;
    v4u r[2] = {v4u{0u, 0u, 0u, 0u}, v4u{0u, 0u, 0u, 0u}};
    u64 m0 = ~0ull, m1 = ~0ull;
#pragma nounroll
    for (int it = 0; it < SPIN_G; ++it) {
      poll2(r, m0, m1, ws, base);
      m0 = stale(r[0], tag);
      m1 = stale(r[1], tag);
      if (!(m0 | m1)) break;
    }
    LAS u8* dst = L + L_Q + (((lane >> 3) & 3) * 64 + ((lane >> 1) & 3) * 16 + cw * 4 + (lane >> 5)) * 16 + (lane & 1) * 8;
    *(LAS v2u*)dst = v2u{r[0].x, r[0].z};
    *(LAS v2u*)(dst + 32) = v2u{r[1].x, r[1].z};
    flag_set(L, F_Q, cw, lane);
    flag_wait4<1>(L, F_Q);
  }
  v4u qf[4];
#pragma unroll
  for (int s = 0; s < 4; ++s) qf[s] = *(LAS v4u*)(L + L_Q + s * 1024 + lane * 16);
  flag_wait1<1>(L, F_K, cw);
  f4 sc[8];
  {
    LAS u8* kr = L + L_K + cw * 32768 + (4 * (c & 3) + (c >> 2)) * 256;
    v4u kf[32];
#pragma unroll
    for (int u = 0; u < 8; ++u)
#pragma unroll
      for (int s = 0; s < 4; ++s) kf[u * 4 + s] = *(LAS v4u*)(kr + u * 4096 + ((4 * s + g) ^ c) * 16);
    SBAR();
#pragma unroll
    for (int u = 0; u < 8; ++u) {
      sc[u] = f4{0.f, 0.f, 0.f, 0.f};
#pragma unroll
      for (int s = 0; s < 4; ++s) sc[u] = mfma(kf[u * 4 + s], qf[s], sc[u]);
    }
  }
  {
    float m = sc[0][0];
#pragma unroll
    for (int u = 0; u < 8; ++u)
#pragma unroll
      for (int r = 0; r < 4; ++r) m = fmaxf(m, sc[u][r]);
    m = xmax(m);
    if (lane < 16) *(LAS float*)(L + L_M + c * 16 + cw * 4) = m;
    flag_set(L, F_M, cw, lane);
    flag_wait4<0>(L, F_M);
    const f4 mm = *(LAS f4*)(L + L_M + c * 16);
    const float M = fmaxf(fmaxf(mm[0], mm[1]), fmaxf(mm[2], mm[3])) * SCL;
    float l = 0.f;
#pragma unroll
    for (int u = 0; u < 8; ++u)
#pragma unroll
      for (int r = 0; r < 4; ++r) {
        sc[u][r] = __builtin_amdgcn_exp2f(__builtin_fmaf(sc[u][r], SCL, -M));
        l += sc[u][r];
      }
    l = xsum(l);
    if (lane < 16) *(LAS float*)(L + L_L + c * 16 + cw * 4) = l;
#pragma unroll
    for (int t = 0; t < 4; ++t) {
      const v4u pv = {pk(sc[2 * t][0], sc[2 * t][1]), pk(sc[2 * t][2], sc[2 * t][3]), pk(sc[2 * t + 1][0], sc[2 * t + 1][1]), pk(sc[2 * t + 1][2], sc[2 * t + 1][3])};
      *(LAS v4u*)(L + L_P + cw * 4096 + t * 1024 + lane * 16) = pv;
    }
    flag_set(L, F_P, cw, lane);
  }
  __builtin_amdgcn_s_setprio(0);
  flag_wait4<1>(L, F_V);
  {
    const __amdgpu_buffer_rsrc_t ro = MKRSRC(wo + (size_t)(kvh * 4 + hl) * 131072, 131072);
    const u32 woo = (cw * 32 + 2 * g) * 1024 + nq * 256 + c * 16;
    if (bh) {
#pragma unroll
      for (int i = 0; i < 8; ++i) { wv[i ^ 4] = __builtin_amdgcn_raw_buffer_load_b128(ro, woo, ((i & 3) * 8 + ((i >> 2) ^ 1)) * 1024, 0); SBAR(); }
    } else {
#pragma unroll
      for (int i = 0; i < 8; ++i) { wv[i] = __builtin_amdgcn_raw_buffer_load_b128(ro, woo, ((i & 3) * 8 + (i >> 2)) * 1024, 0); SBAR(); }
    }
    u32 wd = L_K + cw * 32768 + 16384 + lane * 16;
    PIN(wd);
    SBAR();
#pragma unroll
    for (int e = 0; e < 8; ++e) {
      const v4u fr = tr8(wv, e);
      *(LAS v4u*)(L + wd + e * 1024) = v4u{fr.x, fr.z, fr.y, fr.w};
    }
  }
  flag_set(L, F_W, cw, lane);
  flag_wait4<1>(L, F_W);
  v4u wf[32];
#pragma unroll
  for (int t = 0; t < 4; ++t)
#pragma unroll
    for (int e = 0; e < 8; ++e) wf[t * 8 + e] = *(LAS v4u*)(L + L_K + t * 32768 + 16384 + e * 1024 + lane * 16);
#pragma unroll
  for (int i = 0; i < 32; ++i) PIN(wf[i]);
  {
    const u32 obase = WS_BOX + (kvh * 32 + (bh * 4 + cw) * 4 + hl) * BOX + 8192;
    u32 v0 = obase + (c >> 2) * 2048 + g * 64 + (c & 3) * 16;
    const u32 vs = obase + 8192 + g * 128 + (c & 3) * 16;
    const int u = (cw * 4 + g) * 2;
    u32 off = WS_BOX + ((u >> 2) * 32 + bh * 16 + (u & 3) * 4 + nq) * BOX + (kvh * 4 + hl) * 256 + c * 16;
    PIN(off); PIN(v0);
    v4u st;
#pragma nounroll
    for (int it = 0; it < SPIN_G; ++it) {
      ld1(st, ws, vs);
      if (!stale(st, tag)) break;
      __builtin_amdgcn_s_sleep(1);
    }
    __builtin_amdgcn_s_setprio(3);
    float res[8];
#pragma nounroll
    for (int it = 0; it < SPIN_G; ++it) {
      v4u r[8];
      ld8_issue(r, ws, v0);
      SBAR();
      const float mx = xmax(fb(st.x));
      const float ex = __builtin_amdgcn_exp2f(fb(st.x) - mx);
      const float fq = ex * __builtin_amdgcn_rcpf(xsum(ex * fb(st.z)));
      f4 f = {dppf<0x00>(fq), dppf<0x55>(fq), dppf<0xAA>(fq), dppf<0xFF>(fq)};
      PIN(f);
      ld8_wait(r);
      u32 bad = 0;
#pragma unroll
      for (int t = 0; t < 4; ++t) {
        bad = orxor(orxor(bad, r[2 * t].y, tag), r[2 * t].w, tag);
        r[2 * t].y = r[2 * t + 1].x;
        r[2 * t].w = r[2 * t + 1].z;
      }
#pragma unroll
      for (int e = 0; e < 8; ++e) {
        f4 d = {0.f, 0.f, 0.f, 0.f};
#pragma unroll
        for (int t = 0; t < 4; ++t) d = mfma(r[2 * t], wf[t * 8 + e], d);
        if (e < 4) bad = orxor(orxor(bad, r[2 * e + 1].y, tag), r[2 * e + 1].w, tag);
        res[e] = rsum4(d * f);
      }
      if (!__builtin_amdgcn_ballot_w64(bad != 0u)) break;
    }
    __builtin_amdgcn_raw_buffer_store_b128(v4u{pk(res[0], res[1]), tag, pk(res[2], res[3]), tag}, rs, off, 0, 16);
    __builtin_amdgcn_raw_buffer_store_b128(v4u{pk(res[4], res[5]), tag, pk(res[6], res[7]), tag}, rs, off, 4 * BOX, 16);
    flag_set(L, F_A, cw, lane);
  }
}

extern "C" void kernel_launch(void* const* d_in, const int* in_sizes, int n_in, void* d_out, int out_size, void* d_ws,
                              size_t ws_size, hipStream_t stream) {
  (void)in_sizes; (void)n_in; (void)out_size; (void)ws_size;
  (void)hipFuncSetAttribute((const void*)gqa_fused, hipFuncAttributeMaxDynamicSharedMemorySize, (int)LDS_BYTES);
  hipLaunchKernelGGL(gqa_fused, dim3(256), dim3(512), LDS_BYTES, stream, (const u8*)d_in[0], (const u8*)d_in[1], (const u8*)d_in[2],
                     (const u8*)d_in[3], (const u8*)d_in[4], (u8*)d_out, (u8*)d_ws);
}
```

```cpp
#include <hip/hip_runtime.h>
#include <stdint.h>

typedef unsigned u32;
typedef unsigned long long u64;
typedef unsigned char u8;
typedef u32 v4u __attribute__((ext_vector_type(4)));
typedef u32 v2u __attribute__((ext_vector_type(2)));
typedef float f4 __attribute__((ext_vector_type(4)));
typedef float f2 __attribute__((ext_vector_type(2)));
typedef short s8v __attribute__((ext_vector_type(8)));
typedef __bf16 bf2v __attribute__((ext_vector_type(2)));
#define GAS __attribute__((address_space(1)))
#define LAS __attribute__((address_space(3)))

#define THR 12
#define BURST 4
#define LFIRST 4
#define CFIRST 8
#define QTRIG 28
#define PEND 940
#define PDELTA 138
#define PSTEP BURST
#define FDELAY 12
#define CODEPF 1

constexpr u32 WS_EPOCH = 0;
constexpr u32 WS_QBUF = 4096;
constexpr u32 WS_BOX = WS_QBUF + 64 * 8192;
constexpr u32 BOX = 16896;
constexpr u32 WS_END = WS_BOX + 256 * BOX;
constexpr u32 L_K = 0;
constexpr u32 L_P = 131072;
constexpr u32 L_QP = L_P, L_Q = L_P + 16384;
constexpr u32 L_M = L_Q + 4096, L_L = L_M + 256, L_T = L_L + 256, L_F = L_T + 64;
constexpr u32 LDS_BYTES = L_F + 256;
enum { F_QP = 0, F_Q, F_K, F_M, F_P, F_O, F_V, F_A, F_K2, F_W };

constexpr int SPIN_G = 1 << 17;
constexpr int SPIN_L = 1 << 22;
constexpr float SCL = 0.08838834764831845f * 1.4426950408889634f;

__device__ __forceinline__ f4 mfma(v4u a, v4u b, f4 c) {
  return __builtin_amdgcn_mfma_f32_16x16x32_bf16(__builtin_bit_cast(s8v, a), __builtin_bit_cast(s8v, b), c, 0, 0, 0);
}
__device__ __forceinline__ u32 pk(float lo, float hi) {
  f2 v = {lo, hi};
  return __builtin_bit_cast(u32, __builtin_convertvector(v, bf2v));
}
__device__ __forceinline__ u32 plo(u32 a, u32 b) { return __builtin_amdgcn_perm(b, a, 0x05040100u); }
__device__ __forceinline__ u32 phi(u32 a, u32 b) { return __builtin_amdgcn_perm(b, a, 0x07060302u); }
__device__ __forceinline__ v4u tr8(const v4u* v, int e) {
  v4u r;
#pragma unroll
  for (int jj = 0; jj < 4; ++jj) {
    const u32 a0 = v[2 * jj][e >> 1], a1 = v[2 * jj + 1][e >> 1];
    r[jj] = (e & 1) ? phi(a0, a1) : plo(a0, a1);
  }
  return r;
}
__device__ __forceinline__ float fb(u32 x) { return __builtin_bit_cast(float, x); }
__device__ __forceinline__ u32 bf(float x) { return __builtin_bit_cast(u32, x); }
__device__ __forceinline__ float xmax(float x) {
  auto s = __builtin_amdgcn_permlane16_swap(bf(x), bf(x), false, false);
  x = fmaxf(fb(s[0]), fb(s[1]));
  auto t = __builtin_amdgcn_permlane32_swap(bf(x), bf(x), false, false);
  return fmaxf(fb(t[0]), fb(t[1]));
}
__device__ __forceinline__ float xsum(float x) {
  auto s = __builtin_amdgcn_permlane16_swap(bf(x), bf(x), false, false);
  x = fb(s[0]) + fb(s[1]);
  auto t = __builtin_amdgcn_permlane32_swap(bf(x), bf(x), false, false);
  return fb(t[0]) + fb(t[1]);
}
__device__ __forceinline__ float rsum4(f4 d) {
  auto s02 = __builtin_amdgcn_permlane32_swap(bf(d[0]), bf(d[2]), false, false);
  auto s13 = __builtin_amdgcn_permlane32_swap(bf(d[1]), bf(d[3]), false, false);
  const float x02 = fb(s02[0]) + fb(s02[1]), x13 = fb(s13[0]) + fb(s13[1]);
  auto s = __builtin_amdgcn_permlane16_swap(bf(x02), bf(x13), false, false);
  return fb(s[0]) + fb(s[1]);
}
template <int CTRL> __device__ __forceinline__ float dppf(float x) {
  return __builtin_bit_cast(float, __builtin_amdgcn_mov_dpp(__builtin_bit_cast(int, x), CTRL, 0xf, 0xf, true));
}
__device__ __forceinline__ u64 stale(v4u r, u32 tag) { return __builtin_amdgcn_ballot_w64(((r.y ^ tag) | (r.w ^ tag)) != 0u); }
__device__ __forceinline__ u32 orxor(u32 acc, u32 a, u32 t) { return __builtin_amdgcn_bitop3_b32(acc, a, t, 0xF6); }

__device__ __forceinline__ void poll2(v4u (&r)[2], u64 m0, u64 m1, const u8* base, u32 voff) {
  u64 sv;
  asm volatile("s_mov_b64 %2, exec\n\t"
               "s_mov_b64 exec, %5\n\tglobal_load_dwordx4 %0, %3, %4 sc1\n\t"
               "s_mov_b64 exec, %6\n\tglobal_load_dwordx4 %1, %3, %4 offset:1024 sc1\n\t"
               "s_mov_b64 exec, %2\n\ts_waitcnt vmcnt(0)"
               : "+v"(r[0]), "+v"(r[1]), "=&s"(sv) : "v"(voff), "s"(base), "s"(m0), "s"(m1) : "memory");
}
__device__ __forceinline__ void ld8_issue(v4u (&r)[8], const u8* base, u32 v0) {
  asm volatile("global_load_dwordx4 %0, %8, %9 sc1\n\tglobal_load_dwordx4 %1, %8, %9 offset:256 sc1\n\t"
               "global_load_dwordx4 %2, %8, %9 offset:512 sc1\n\tglobal_load_dwordx4 %3, %8, %9 offset:768 sc1\n\t"
               "global_load_dwordx4 %4, %8, %9 offset:1024 sc1\n\tglobal_load_dwordx4 %5, %8, %9 offset:1280 sc1\n\t"
               "global_load_dwordx4 %6, %8, %9 offset:1536 sc1\n\tglobal_load_dwordx4 %7, %8, %9 offset:1792 sc1"
               : "=&v"(r[0]), "=&v"(r[1]), "=&v"(r[2]), "=&v"(r[3]), "=&v"(r[4]), "=&v"(r[5]), "=&v"(r[6]), "=&v"(r[7])
               : "v"(v0), "s"(base) : "memory");
}
__device__ __forceinline__ void ld8_wait(v4u (&r)[8]) {
  asm volatile("s_waitcnt vmcnt(0)"
               : "+v"(r[0]), "+v"(r[1]), "+v"(r[2]), "+v"(r[3]), "+v"(r[4]), "+v"(r[5]), "+v"(r[6]), "+v"(r[7]) :: "memory");
}
__device__ __forceinline__ void fetch8(v4u (&r)[8], u32 want, const u8* base, u32 v0, u32 v1) {
  asm volatile("s_bitcmp1_b32 %11, 0\n\ts_cbranch_scc0 1f\n\tglobal_load_dwordx4 %0, %8, %10 sc1\n"
               "1:\n\ts_bitcmp1_b32 %11, 1\n\ts_cbranch_scc0 2f\n\tglobal_load_dwordx4 %1, %8, %10 offset:1024 sc1\n"
               "2:\n\ts_bitcmp1_b32 %11, 2\n\ts_cbranch_scc0 3f\n\tglobal_load_dwordx4 %2, %8, %10 offset:2048 sc1\n"
               "3:\n\ts_bitcmp1_b32 %11, 3\n\ts_cbranch_scc0 4f\n\tglobal_load_dwordx4 %3, %8, %10 offset:3072 sc1\n"
               "4:\n\ts_bitcmp1_b32 %11, 4\n\ts_cbranch_scc0 5f\n\tglobal_load_dwordx4 %4, %9, %10 sc1\n"
               "5:\n\ts_bitcmp1_b32 %11, 5\n\ts_cbranch_scc0 6f\n\tglobal_load_dwordx4 %5, %9, %10 offset:1024 sc1\n"
               "6:\n\ts_bitcmp1_b32 %11, 6\n\ts_cbranch_scc0 7f\n\tglobal_load_dwordx4 %6, %9, %10 offset:2048 sc1\n"
               "7:\n\ts_bitcmp1_b32 %11, 7\n\ts_cbranch_scc0 8f\n\tglobal_load_dwordx4 %7, %9, %10 offset:3072 sc1\n"
               "8:\n\ts_waitcnt vmcnt(0)"
               : "+v"(r[0]), "+v"(r[1]), "+v"(r[2]), "+v"(r[3]), "+v"(r[4]), "+v"(r[5]), "+v"(r[6]), "+v"(r[7])
               : "v"(v0), "v"(v1), "s"(base), "s"(want) : "memory", "scc");
}
__device__ __forceinline__ void ld1(v4u& r, const u8* base, u32 v0) {
  asm volatile("global_load_dwordx4 %0, %1, %2 sc1\n\ts_waitcnt vmcnt(0)" : "=&v"(r) : "v"(v0), "s"(base) : "memory");
}
#define CBAR() asm volatile("" ::: "memory")
#define SBAR() __builtin_amdgcn_sched_barrier(0)
#define PIN(x) asm volatile("" : "+v"(x))
#define WAITVM(n) asm volatile("s_waitcnt vmcnt(%0)" ::"i"(n) : "memory")

__device__ __forceinline__ void pace(u64 tz, int ticks) {
#pragma nounroll
  while ((int)(u32)(__builtin_amdgcn_s_memrealtime() - tz) < ticks) __builtin_amdgcn_s_sleep(2);
}
__device__ __forceinline__ void flag_set(LAS u8* L, int f, int w, int lane) {
  CBAR();
  if (lane == 0) *(volatile LAS u32*)(L + L_F + f * 16 + w * 4) = 1u;
  CBAR();
}
template <int SLEEP> __device__ __forceinline__ void flag_wait4(LAS u8* L, int f) {
#pragma nounroll
  for (int i = 0; i < SPIN_L; ++i) {
    v4u v = *(volatile LAS v4u*)(L + L_F + f * 16);
    if (__builtin_amdgcn_readfirstlane(v.x & v.y & v.z & v.w)) break;
    if (SLEEP) __builtin_amdgcn_s_sleep(SLEEP);
  }
  CBAR();
}
template <int SLEEP> __device__ __forceinline__ void flag_wait1(LAS u8* L, int f, int w) {
#pragma nounroll
  for (int i = 0; i < SPIN_L; ++i) {
    u32 v = *(volatile LAS u32*)(L + L_F + f * 16 + w * 4);
    if (__builtin_amdgcn_readfirstlane(v)) break;
    if (SLEEP) __builtin_amdgcn_s_sleep(SLEEP);
  }
  CBAR();
}
#define MKRSRC(p, n) __builtin_amdgcn_make_buffer_rsrc((void*)(p), 0, (int)(n), 0x00020000)

__global__ void __launch_bounds__(512, 2)
gqa_fused(const u8* x, const u8* kc, const u8* vc, const u8* wq, const u8* wo, u8* out, u8* ws) {
  extern __shared__ __attribute__((aligned(16))) u8 smem[];
  LAS u8* L = (LAS u8*)smem;
  const int tid = threadIdx.x, lane = tid & 63, wave = __builtin_amdgcn_readfirstlane(tid >> 6);
  const int g = lane >> 4, c = lane & 15;
  const int bid = blockIdx.x, kvh = bid & 7, j = bid >> 3, b = j >> 2, split = j & 3;
  const int wg = kvh * 32 + j;
  const int bh = j >> 4, hl = (j >> 2) & 3, nq = j & 3;

  if (wave < 4) {
    const int w = wave;
    const u64 tz = __builtin_amdgcn_s_memrealtime();
    const int r8 = (w + 4 * (j & 1) + (j >> 1)) & 7;
    const size_t kvoff = ((size_t)((b * 8 + kvh) * 2048 + split * 512 + w * 128)) * 256;
    const u8* kb = kc + kvoff;
    const __amdgpu_buffer_rsrc_t rv = MKRSRC(vc + kvoff, 32768);
    u32 ko[4];
#pragma unroll
    for (int i = 0; i < 4; ++i) ko[i] = g * 256 + ((c ^ (4 * g + i)) * 16);
    const u32 ldsk = (u32)(size_t)L + L_K + w * 32768;
#pragma unroll
    for (int q4 = 0; q4 < 8; ++q4) {
#pragma unroll
      for (int i = 0; i < 4; ++i) {
        const int n = q4 * 4 + i;
        if (PEND && n % PSTEP == 0 && n >= 16) pace(tz, PEND - (63 - n) * PDELTA / 10);
        if (n % BURST == 0 && n + BURST > THR) WAITVM(THR - BURST);
        u32 keep;
        asm volatile("s_mov_b32 %0, m0\n\ts_mov_b32 m0, %3\n\ts_nop 0\n\tglobal_load_lds_dwordx4 %1, %2 offset:%4 nt\n\ts_mov_b32 m0, %0"
                     : "=&s"(keep) : "v"(ko[i]), "s"(kb + ((q4 + r8) & 7) * 4096), "s"(ldsk + q4 * 4096), "i"(i * 1024) : "memory");
        if (n == QTRIG) flag_set(L, F_K2, w, lane);
        if (n == LFIRST - 1) {
          if (w == 0) {
            *(volatile LAS u32*)(L + L_F + lane * 4) = 0u;
            asm volatile("s_waitcnt lgkmcnt(0)" ::: "memory");
          }
          SBAR();
          __builtin_amdgcn_s_barrier();
          SBAR();
        }
      }
    }
    v4u v[32];
#pragma unroll
    for (int i = 0; i < 32; ++i) {
      if (PEND && i % PSTEP == 0) pace(tz, PEND - (31 - i) * PDELTA / 10);
      if (i % BURST == 0) {
        WAITVM(THR - BURST);
        if (i >= THR - BURST && i - BURST < THR - BURST) { flag_set(L, F_K, w, lane); }
      }
      v[i] = __builtin_amdgcn_raw_buffer_load_b128(rv, lane * 16 + (i & 3) * 1024, (((i >> 2) + r8) & 7) * 4096, 2);
    }
    SBAR();
    flag_set(L, F_V, w, lane);
    const __amdgpu_buffer_rsrc_t rs = MKRSRC(ws, WS_END);
    flag_wait4<1>(L, F_P);
    __builtin_amdgcn_s_setprio(3);
    v4u p[4];
#pragma unroll
    for (int t = 0; t < 4; ++t) p[t] = *(LAS v4u*)(L + L_P + w * 4096 + t * 1024 + lane * 16);
    const u32 tag = __builtin_amdgcn_readfirstlane(*(LAS u32*)(L + L_T));
    v4u stv;
    {
      const f4 mm = *(LAS f4*)(L + L_M + (4 * w + (lane & 3)) * 16), ll = *(LAS f4*)(L + L_L + (4 * w + (lane & 3)) * 16);
      stv = v4u{bf(fmaxf(fmaxf(mm[0], mm[1]), fmaxf(mm[2], mm[3])) * SCL), tag, bf((ll[0] + ll[1]) + (ll[2] + ll[3])), tag};
    }
    const u32 obase = WS_BOX + (kvh * 32 + b * 4 + w) * BOX + 8192;
    u32 ooff = obase + split * 2048 + lane * 16;
    u32 soff = lane < 4 ? obase + 8192 + split * 128 + lane * 16 : 0x7FFFFFF0u;
    u32 rsrc_ = L_K + ((lane >> 2) & 3) * 2112 + ((lane >> 4) & 1) * 1024 + ((lane >> 5) * 16 + 4 * w + (lane & 3)) * 16;
    u32 wdst_ = L_K + w * 32768 + lane * 16;
    PIN(rsrc_); PIN(wdst_); PIN(ooff); PIN(soff); PIN(stv);
    f4 o[8];
#pragma unroll
    for (int e = 0; e < 8; ++e) o[e] = f4{0.f, 0.f, 0.f, 0.f};
#pragma unroll
    for (int t = 0; t < 4; ++t) {
#pragma unroll
      for (int e = 0; e < 8; ++e) o[e] = mfma(tr8(&v[8 * t], e), p[t], o[e]);
      if (t < 3) SBAR();
    }
#pragma unroll
    for (int e = 0; e < 8; ++e) *(LAS f4*)(L + wdst_ + (e >> 1) * 2112 + (e & 1) * 1024) = o[e];
    flag_set(L, F_O, w, lane);
    flag_wait4<0>(L, F_O);
    {
      LAS u8* src = L + rsrc_;
      f4 s0 = *(LAS f4*)src, s1 = *(LAS f4*)(src + 512);
#pragma unroll
      for (int ww = 1; ww < 4; ++ww) {
        s0 += *(LAS f4*)(src + ww * 32768);
        s1 += *(LAS f4*)(src + ww * 32768 + 512);
      }
      __builtin_amdgcn_raw_buffer_store_b128(v4u{pk(s0[0], s0[1]), tag, pk(s0[2], s0[3]), tag}, rs, ooff, 0, 16);
      __builtin_amdgcn_raw_buffer_store_b128(v4u{pk(s1[0], s1[1]), tag, pk(s1[2], s1[3]), tag}, rs, ooff + 1024, 0, 16);
      SBAR();
      __builtin_amdgcn_raw_buffer_store_b128(stv, rs, soff, 0, 16);
    }
    if (w == 0) {
      __builtin_amdgcn_s_setprio(0);
      const u32 base = WS_BOX + wg * BOX + lane * 16;
      const int u = kvh * 4 + hl, row16 = u >> 1, eh = u & 1;
      u32 oo = ((bh * 16 + row16) * 512 + nq * 128 + 8 * c + 4 * eh + g) * 2;
      PIN(oo);
      v4u r[8];
#pragma unroll
      for (int k = 0; k < 8; ++k) r[k] = v4u{0u, 0u, 0u, 0u};
      u32 todo = 255u;
      flag_wait4<1>(L, F_A);
      __builtin_amdgcn_s_setprio(3);
      __builtin_amdgcn_s_sleep(FDELAY);
      f4 s;
#pragma nounroll
      for (int it = 0; it < SPIN_G; ++it) {
        fetch8(r, (u32)__builtin_amdgcn_readfirstlane(todo), ws, base, base + 4096);
        u32 bk[8];
#pragma unroll
        for (int k = 0; k < 8; ++k) bk[k] = orxor(r[k].y ^ tag, r[k].w, tag);
        const u32 bad = ((bk[0] | bk[1]) | (bk[2] | bk[3] | bk[4])) | (bk[5] | bk[6] | bk[7]);
        f4 h[4];
#pragma unroll
        for (int k = 0; k < 4; ++k)
          h[k] = f4{fb(r[2 * k].x << 16), fb(r[2 * k].x & 0xFFFF0000u), fb(r[2 * k].z << 16), fb(r[2 * k].z & 0xFFFF0000u)} +
                 f4{fb(r[2 * k + 1].x << 16), fb(r[2 * k + 1].x & 0xFFFF0000u), fb(r[2 * k + 1].z << 16), fb(r[2 * k + 1].z & 0xFFFF0000u)};
        s = (h[0] + h[1]) + (h[2] + h[3]);
        PIN(s);
        if (!__builtin_amdgcn_ballot_w64(bad != 0u)) break;
        u32 nt = 0;
#pragma unroll
        for (int k = 0; k < 8; ++k) nt |= __builtin_amdgcn_ballot_w64(bk[k] != 0u) ? (1u << k) : 0u;
        todo = nt;
      }
      const float tot = rsum4(s);
      const __bf16 ob = (__bf16)tot;
      *(GAS unsigned short*)(out + oo) = __builtin_bit_cast(unsigned short, ob);
      if (kvh == 0 && (j & 15) == 0 && lane == 0) __builtin_amdgcn_raw_buffer_store_b32(tag, rs, WS_EPOCH + bh * 256, 0, 16);
    }
    asm volatile("gqa_code_mark:" ::: "memory");
    return;
  }

  const int cw = wave - 4, ct = tid - 256;
  if (cw == 3) {
    u64 pc;
    int len;
    asm volatile("s_getpc_b64 %0\n1:\n\ts_mov_b32 %1, gqa_code_mark-1b" : "=s"(pc), "=s"(len));
    const u32 lim = (u32)(len < 16 ? 16 : len) - 16u;
    u32 keep;
    asm volatile("s_mov_b32 %0, m0\n\ts_mov_b32 m0, %3\n\ts_nop 0\n\tglobal_load_lds_dwordx4 %1, %2\n\ts_mov_b32 m0, %0"
                 : "=&s"(keep) : "v"(min((u32)((j & 15) * 1024 + lane * 16), lim) & ~15u), "s"((const u8*)(pc & ~127ull)), "s"((u32)(size_t)L + L_Q) : "memory");
  }
  const __amdgpu_buffer_rsrc_t rs = MKRSRC(ws, WS_END);
  __builtin_amdgcn_s_setprio(3);
  const int qi = j & 7;
  const int qrot = (j >> 3) * 4;
  u32 tag;
  v4u wv[16];
  {
    v4u xa[2];
    const u32 ep = __builtin_amdgcn_raw_buffer_load_b32(rs, ct == 0 ? WS_EPOCH + bh * 256 : 0x7FFFFFF0u, 0, 16);
    const __amdgpu_buffer_rsrc_t rx = MKRSRC(x, 32768);
    const __amdgpu_buffer_rsrc_t rq = MKRSRC(wq, 4u << 20);
    const u32 xo = (((b & ~1) * 4 + (c & 7)) * 512 + cw * 128 + (2 * g + (c >> 3)) * 8) * 2;
#pragma unroll
    for (int T = 0; T < 2; ++T) xa[T] = __builtin_amdgcn_raw_buffer_load_b128(rx, xo, (T ^ (qrot >> 3)) * 128, 0);
    SBAR();
    const u32 wqo = ((cw * 128 + (lane >> 3) * 8) * 4096 + kvh * 512 + qi * 64 + (lane & 7) * 8) * 2;
#pragma unroll
    for (int i = 0; i < 16; ++i) {
      const int ii = i ^ qrot;
      wv[i] = __builtin_amdgcn_raw_buffer_load_b128(rq, wqo, ((ii >> 3) * 64 + (ii & 7)) * 8192, 0);
      if (i == CFIRST - 1) { SBAR(); __builtin_amdgcn_s_barrier(); SBAR(); }
    }
    SBAR();
    if (qrot & 4) {
#pragma unroll
      for (int T = 0; T < 2; ++T) xa[T] = v4u{xa[T].z, xa[T].w, xa[T].x, xa[T].y};
    }
    f4 qa[8];
#pragma unroll
    for (int e = 0; e < 8; ++e) qa[e] = f4{0.f, 0.f, 0.f, 0.f};
#pragma unroll
    for (int T = 0; T < 2; ++T)
#pragma unroll
      for (int e = 0; e < 8; ++e) qa[e] = mfma(xa[T], tr8(&wv[8 * T], e), qa[e]);
    if ((g >> 1) == (c >> 3)) {
#pragma unroll
      for (int r = 0; r < 4; ++r) {
        LAS u8* dst = L + L_QP + cw * 4096 + (g * 4 + r) * 256 + (c & 7) * 32;
        *(LAS f4*)dst = f4{qa[0][r], qa[1][r], qa[2][r], qa[3][r]};
        *(LAS f4*)(dst + 16) = f4{qa[4][r], qa[5][r], qa[6][r], qa[7][r]};
      }
    }
    if (ct == 0) *(LAS u32*)(L + L_T) = ep + 1u;
    flag_set(L, F_QP, cw, lane);
    flag_wait4<0>(L, F_QP);
    tag = __builtin_amdgcn_readfirstlane(*(LAS u32*)(L + L_T));
    if (cw < 2) {
      const int row = ct >> 4;
      f4 s = {0.f, 0.f, 0.f, 0.f};
#pragma unroll
      for (int ww = 0; ww < 8; ++ww) s += *(LAS f4*)(L + L_QP + ww * 2048 + row * 256 + c * 16);
      const u32 off = WS_QBUF + ((((b & ~1) + (row >> 2)) * 8 + kvh) * 16 + (qi >> 1) * 4 + (row & 3)) * 512 + (qi & 1) * 256 + c * 16;
      __builtin_amdgcn_raw_buffer_store_b128(v4u{pk(s[0], s[1]), tag, pk(s[2], s[3]), tag}, rs, off, 0, 16);
    }
    SBAR();
  }
  flag_wait1<4>(L, F_K2, cw);
  {
    const u32 base = WS_QBUF + ((b * 8 + kvh) * 16 + cw * 4) * 512 + lane * 16;
    v4u r[2] = {v4u{0u, 0u, 0u, 0u}, v4u{0u, 0u, 0u, 0u}};
    u64 m0 = ~0ull, m1 = ~0ull;
#pragma nounroll
    for (int it = 0; it < SPIN_G; ++it) {
      poll2(r, m0, m1, ws, base);
      m0 = stale(r[0], tag);
      m1 = stale(r[1], tag);
      if (!(m0 | m1)) break;
    }
    LAS u8* dst = L + L_Q + (((lane >> 3) & 3) * 64 + ((lane >> 1) & 3) * 16 + cw * 4 + (lane >> 5)) * 16 + (lane & 1) * 8;
    *(LAS v2u*)dst = v2u{r[0].x, r[0].z};
    *(LAS v2u*)(dst + 32) = v2u{r[1].x, r[1].z};
    flag_set(L, F_Q, cw, lane);
    flag_wait4<1>(L, F_Q);
  }
  v4u qf[4];
#pragma unroll
  for (int s = 0; s < 4; ++s) qf[s] = *(LAS v4u*)(L + L_Q + s * 1024 + lane * 16);
  flag_wait1<1>(L, F_K, cw);
  f4 sc[8];
  {
    LAS u8* kr = L + L_K + cw * 32768 + (4 * (c & 3) + (c >> 2)) * 256;
    v4u kf[32];
#pragma unroll
    for (int u = 0; u < 8; ++u)
#pragma unroll
      for (int s = 0; s < 4; ++s) kf[u * 4 + s] = *(LAS v4u*)(kr + u * 4096 + ((4 * s + g) ^ c) * 16);
    SBAR();
#pragma unroll
    for (int u = 0; u < 8; ++u) {
      sc[u] = f4{0.f, 0.f, 0.f, 0.f};
#pragma unroll
      for (int s = 0; s < 4; ++s) sc[u] = mfma(kf[u * 4 + s], qf[s], sc[u]);
    }
  }
  {
    float m = sc[0][0];
#pragma unroll
    for (int u = 0; u < 8; ++u)
#pragma unroll
      for (int r = 0; r < 4; ++r) m = fmaxf(m, sc[u][r]);
    m = xmax(m);
    if (lane < 16) *(LAS float*)(L + L_M + c * 16 + cw * 4) = m;
    flag_set(L, F_M, cw, lane);
    flag_wait4<0>(L, F_M);
    const f4 mm = *(LAS f4*)(L + L_M + c * 16);
    const float M = fmaxf(fmaxf(mm[0], mm[1]), fmaxf(mm[2], mm[3])) * SCL;
    float l = 0.f;
#pragma unroll
    for (int u = 0; u < 8; ++u)
#pragma unroll
      for (int r = 0; r < 4; ++r) {
        sc[u][r] = __builtin_amdgcn_exp2f(__builtin_fmaf(sc[u][r], SCL, -M));
        l += sc[u][r];
      }
    l = xsum(l);
    if (lane < 16) *(LAS float*)(L + L_L + c * 16 + cw * 4) = l;
#pragma unroll
    for (int t = 0; t < 4; ++t) {
      const v4u pv = {pk(sc[2 * t][0], sc[2 * t][1]), pk(sc[2 * t][2], sc[2 * t][3]), pk(sc[2 * t + 1][0], sc[2 * t + 1][1]), pk(sc[2 * t + 1][2], sc[2 * t + 1][3])};
      *(LAS v4u*)(L + L_P + cw * 4096 + t * 1024 + lane * 16) = pv;
    }
    flag_set(L, F_P, cw, lane);
  }
  __builtin_amdgcn_s_setprio(0);
  flag_wait4<1>(L, F_V);
  {
    const __amdgpu_buffer_rsrc_t ro = MKRSRC(wo + (size_t)(kvh * 4 + hl) * 131072, 131072);
    const u32 woo = (cw * 32 + 2 * g) * 1024 + nq * 256 + c * 16;
    if (bh) {
#pragma unroll
      for (int i = 0; i < 8; ++i) { wv[i ^ 4] = __builtin_amdgcn_raw_buffer_load_b128(ro, woo, ((i & 3) * 8 + ((i >> 2) ^ 1)) * 1024, 0); SBAR(); }
    } else {
#pragma unroll
      for (int i = 0; i < 8; ++i) { wv[i] = __builtin_amdgcn_raw_buffer_load_b128(ro, woo, ((i & 3) * 8 + (i >> 2)) * 1024, 0); SBAR(); }
    }
    u32 wd = L_K + cw * 32768 + 16384 + lane * 16;
    PIN(wd);
    SBAR();
#pragma unroll
    for (int e = 0; e < 8; ++e) {
      const v4u fr = tr8(wv, e);
      *(LAS v4u*)(L + wd + e * 1024) = v4u{fr.x, fr.z, fr.y, fr.w};
    }
  }
  flag_set(L, F_W, cw, lane);
  flag_wait4<1>(L, F_W);
  v4u wf[32];
#pragma unroll
  for (int t = 0; t < 4; ++t)
#pragma unroll
    for (int e = 0; e < 8; ++e) wf[t * 8 + e] = *(LAS v4u*)(L + L_K + t * 32768 + 16384 + e * 1024 + lane * 16);
#pragma unroll
  for (int i = 0; i < 32; ++i) PIN(wf[i]);
  {
    const u32 obase = WS_BOX + (kvh * 32 + (bh * 4 + cw) * 4 + hl) * BOX + 8192;
    u32 v0 = obase + (c >> 2) * 2048 + g * 64 + (c & 3) * 16;
    const u32 vs = obase + 8192 + g * 128 + (c & 3) * 16;
    const int u = (cw * 4 + g) * 2;
    u32 off = WS_BOX + ((u >> 2) * 32 + bh * 16 + (u & 3) * 4 + nq) * BOX + (kvh * 4 + hl) * 256 + c * 16;
    u32 off2 = off + 4 * BOX;
    PIN(off); PIN(off2); PIN(v0);
    v4u st;
#pragma nounroll
    for (int it = 0; it < SPIN_G; ++it) {
      ld1(st, ws, vs);
      if (!stale(st, tag)) break;
      __builtin_amdgcn_s_sleep(1);
    }
    __builtin_amdgcn_s_setprio(3);
    float res[8];
#pragma nounroll
    for (int it = 0; it < SPIN_G; ++it) {
      v4u r[8];
      ld8_issue(r, ws, v0);
      SBAR();
      const float mx = xmax(fb(st.x));
      const float ex = __builtin_amdgcn_exp2f(fb(st.x) - mx);
      const float fq = ex * __builtin_amdgcn_rcpf(xsum(ex * fb(st.z)));
      f4 f = {dppf<0x00>(fq), dppf<0x55>(fq), dppf<0xAA>(fq), dppf<0xFF>(fq)};
      PIN(f);
      ld8_wait(r);
      u32 bad = 0;
#pragma unroll
      for (int t = 0; t < 4; ++t) {
        bad = orxor(orxor(bad, r[2 * t].y, tag), r[2 * t].w, tag);
        r[2 * t].y = r[2 * t + 1].x;
        r[2 * t].w = r[2 * t + 1].z;
      }
#pragma unroll
      for (int e = 0; e < 8; ++e) {
        f4 d = {0.f, 0.f, 0.f, 0.f};
#pragma unroll
        for (int t = 0; t < 4; ++t) d = mfma(r[2 * t], wf[t * 8 + e], d);
        if (e < 4) bad = orxor(orxor(bad, r[2 * e + 1].y, tag), r[2 * e + 1].w, tag);
        res[e] = rsum4(d * f);
      }
      if (!__builtin_amdgcn_ballot_w64(bad != 0u)) break;
    }
    __builtin_amdgcn_raw_buffer_store_b128(v4u{pk(res[0], res[1]), tag, pk(res[2], res[3]), tag}, rs, off, 0, 16);
    __builtin_amdgcn_raw_buffer_store_b128(v4u{pk(res[4], res[5]), tag, pk(res[6], res[7]), tag}, rs, off2, 0, 16);
    flag_set(L, F_A, cw, lane);
  }
}

extern "C" void kernel_launch(void* const* d_in, const int* in_sizes, int n_in, void* d_out, int out_size, void* d_ws,
                              size_t ws_size, hipStream_t stream) {
  (void)in_sizes; (void)n_in; (void)out_size; (void)ws_size;
  (void)hipFuncSetAttribute((const void*)gqa_fused, hipFuncAttributeMaxDynamicSharedMemorySize, (int)LDS_BYTES);
  hipLaunchKernelGGL(gqa_fused, dim3(256), dim3(512), LDS_BYTES, stream, (const u8*)d_in[0], (const u8*)d_in[1], (const u8*)d_in[2],
                     (const u8*)d_in[3], (const u8*)d_in[4], (u8*)d_out, (u8*)d_ws);
}
```
